```python
import torch

def _softmax(x, axis=-1):
    return torch.softmax(x, dim=axis)
def _gelu(x):
    return torch.nn.functional.gelu(x, approximate="tanh")
def _silu(x):
    return torch.nn.functional.silu(x)
def _sigmoid(x):
    return torch.sigmoid(x)
def _layernorm(x, axis=-1, eps=1e-5):
    m = x.mean(dim=axis, keepdim=True)
    v = x.var(dim=axis, keepdim=True, unbiased=False)
    return (x - m) / torch.sqrt(v + eps)
def _scatter_add(tgt, idx, src, axis=0):
    return tgt.clone().index_add_(axis, idx, src)
def _segment_sum(src, seg, n):
    out = torch.zeros((n,) + src.shape[1:], dtype=src.dtype)
    return out.index_add_(0, seg, src)
def _pad(x, pw):
    flat = tuple(v for pair in reversed(pw) for v in pair)
    return torch.nn.functional.pad(x, flat)

kernel_name = 'parallel_sum__ssm_conv_s4d__mixer_token_mlp__prompt'

def setup_inputs():

    _w = (lambda a: (lambda t: t.to(torch.bfloat16) if t.is_floating_point() else t)(torch.as_tensor(a)))
    def _A_setup():
        Bsz, L, D, N = 1, 2048, 512, 128
        return {
            "u": _w((lambda *s: torch.randn(*s, dtype=torch.bfloat16) * 0.05)(Bsz, L, D)),
            "log_A_real": _w((lambda *s: torch.randn(*s, dtype=torch.bfloat16) * 3.2)(D, N) * 0.5),
            "A_imag": _w((lambda *s: torch.randn(*s, dtype=torch.bfloat16) * 0.05)(D, N) * 3.14),
            "C_real": _w((lambda *s: torch.randn(*s, dtype=torch.bfloat16) * 0.2)(D, N) / N ** 0.5),
            "C_imag": _w((lambda *s: torch.randn(*s, dtype=torch.bfloat16) * 0.4)(D, N) / N ** 0.5),
            "log_dt": _w((lambda *s: torch.randn(*s, dtype=torch.bfloat16) * 0.05)(D) * 0.5 - 4.0),
            "D_skip": _w((lambda *s: torch.randn(*s, dtype=torch.bfloat16) * 0.05)(D)),
        }
    _A_in = _A_setup()

    def _B_setup():
        B, S, D, F = 1, 2048, 512, 1792
        return {
            "x": _w((lambda *s: torch.randn(*s, dtype=torch.bfloat16) * 0.05)(B, S, D)),
            "w1": _w((lambda *s: torch.randn(*s, dtype=torch.bfloat16) * 0.8)(S, F) * (S ** -0.5)),
            "b1": _w((lambda *s: torch.randn(*s, dtype=torch.bfloat16) * 3.2)(F) * 0.02),
            "w2": _w((lambda *s: torch.randn(*s, dtype=torch.bfloat16) * 0.8)(F, S) * (F ** -0.5)),
            "b2": _w((lambda *s: torch.randn(*s, dtype=torch.bfloat16) * 3.2)(S) * 0.02),
        }
    _B_in = _B_setup()

    return {
        "A_u": _A_in["u"],
        "A_log_A_real": _A_in["log_A_real"],
        "A_A_imag": _A_in["A_imag"],
        "A_C_real": _A_in["C_real"],
        "A_C_imag": _A_in["C_imag"],
        "A_log_dt": _A_in["log_dt"],
        "A_D_skip": _A_in["D_skip"],
        "B_x": _B_in["x"],
        "B_w1": _B_in["w1"],
        "B_b1": _B_in["b1"],
        "B_w2": _B_in["w2"],
        "B_b2": _B_in["b2"],
    }

def reference(A_u, A_log_A_real, A_A_imag, A_C_real, A_C_imag, A_log_dt, A_D_skip, B_x, B_w1, B_b1, B_w2, B_b2):
    def _A_ref(u, log_A_real, A_imag, C_real, C_imag, log_dt, D_skip):
        L = u.shape[1]
        dt = torch.exp(log_dt)
        A = -torch.exp(log_A_real) + 1j * A_imag
        C = (C_real + 1j * C_imag) * dt[:, None]
        pos = (lambda x, dt: x.to(dt))(torch.arange(L), log_dt.dtype)
        P = torch.exp((A * dt[:, None])[:, :, None] * pos[None, None, :])
        K = 2.0 * torch.einsum("dn,dnl->dl", C, P).real
        L2 = 2 * L
        u_t = (lambda x, *d: x.permute(*d))(u, 0, 2, 1)
        Kf = (lambda x, *a, **k: torch.fft.rfft(x.float(), *a, **k))(K, L2)
        Uf = (lambda x, *a, **k: torch.fft.rfft(x.float(), *a, **k))(u_t, L2)
        y = (lambda x, *a, **k: torch.fft.irfft(x, *a, **k).to(torch.bfloat16))(Uf * Kf[None, :, :], L2)[:, :, :L]
        y = (lambda x, *d: x.permute(*d))(y, 0, 2, 1)
        return y + u * D_skip[None, None, :]

    def _B_ref(x, w1, b1, w2, b2):
        B, S, D = x.shape
        h = _layernorm(x, axis=-1)
        u = torch.einsum("bsd,sf->bdf", h, w1) + b1
        u = _gelu(u)
        y = torch.einsum("bdf,fs->bds", u, w2) + b2
        return (lambda x, *d: x.permute(*d))(y, 0, 2, 1)

    x = A_u
    xn = _layernorm(x, axis=-1)
    return x + _A_ref(xn, A_log_A_real, A_A_imag, A_C_real, A_C_imag, A_log_dt, A_D_skip) + _B_ref(xn, B_w1, B_b1, B_w2, B_b2)
```

```cpp
#include <hip/hip_runtime.h>
#include <hip/hip_bf16.h>
#include <stdint.h>

typedef __attribute__((ext_vector_type(8))) short bf16x8;
typedef __attribute__((ext_vector_type(4))) short bf16x4;
typedef __attribute__((ext_vector_type(4))) float f32x4;
typedef unsigned short u16;

#define L_SEQ 2048
#define D_CH 512
#define N_ST 128
#define F_FF 1792

__device__ __forceinline__ float bf2f(u16 v) { return __uint_as_float(((unsigned)v) << 16); }
__device__ __forceinline__ u16 f2bf(float f) {
  unsigned u = __float_as_uint(f);
  u += 0x7fffu + ((u >> 16) & 1u);
  return (u16)(u >> 16);
}
__device__ __forceinline__ float rbf(float f) { return bf2f(f2bf(f)); }

__device__ __forceinline__ float wave_sum(float v) {
#pragma unroll
  for (int o = 32; o > 0; o >>= 1) v += __shfl_xor(v, o, 64);
  return v;
}

__device__ __forceinline__ void ln8(const float* x, float* y) {
  float s = 0.f;
#pragma unroll
  for (int i = 0; i < 8; ++i) s += x[i];
  s = wave_sum(s);
  float mean = s * (1.0f / 512.0f);
  float q = 0.f;
#pragma unroll
  for (int i = 0; i < 8; ++i) { float d = x[i] - mean; q += d * d; }
  q = wave_sum(q);
  float var = q * (1.0f / 512.0f);
  float m_b = rbf(mean);
  float v_b = rbf(var);
  float den = rbf(sqrtf(rbf(v_b + 1e-5f)));
#pragma unroll
  for (int i = 0; i < 8; ++i) y[i] = rbf(rbf(x[i] - m_b) / den);
}

__global__ __launch_bounds__(256) void ln_kernel(const u16* __restrict__ x, u16* __restrict__ xn,
                                                 u16* __restrict__ h, u16* __restrict__ xnT) {
  __shared__ u16 tr[32][512 + 8];
  const int wave = threadIdx.x >> 6, lane = threadIdx.x & 63;
  const int s0 = blockIdx.x * 32;
  for (int rr = 0; rr < 8; ++rr) {
    const int r = wave * 8 + rr;
    const int s = s0 + r;
    bf16x8 v = *(const bf16x8*)(x + (size_t)s * 512 + lane * 8);
    float xf[8], a[8], b[8];
#pragma unroll
    for (int i = 0; i < 8; ++i) xf[i] = bf2f((u16)v[i]);
    ln8(xf, a);
    ln8(a, b);
    bf16x8 oa, ob;
#pragma unroll
    for (int i = 0; i < 8; ++i) { oa[i] = (short)f2bf(a[i]); ob[i] = (short)f2bf(b[i]); }
    *(bf16x8*)(xn + (size_t)s * 512 + lane * 8) = oa;
    *(bf16x8*)(h + (size_t)s * 512 + lane * 8) = ob;
#pragma unroll
    for (int i = 0; i < 8; ++i) tr[r][lane * 8 + i] = (u16)oa[i];
  }
  __syncthreads();
  for (int idx = threadIdx.x; idx < 512 * 4; idx += 256) {
    const int d = idx >> 2, c = idx & 3;
    bf16x8 o;
#pragma unroll
    for (int i = 0; i < 8; ++i) o[i] = (short)tr[c * 8 + i][d];
    *(bf16x8*)(xnT + (size_t)d * 2048 + s0 + c * 8) = o;
  }
}

__device__ __forceinline__ int pos_of_idx(int idx) {
  if (idx < 256) return idx;
  if (idx < 384) return 256 + 2 * (idx - 256);
  if (idx < 512) return 512 + 4 * (idx - 384);
  return 1024 + 8 * (idx - 512);
}
__device__ __forceinline__ int idx_of_l(int l) {
  int p = (int)rbf((float)l);
  if (p < 256) return p;
  if (p < 512) return 256 + ((p - 256) >> 1);
  if (p < 1024) return 384 + ((p - 512) >> 2);
  return 512 + ((p - 1024) >> 3);
}

__global__ __launch_bounds__(256) void conv_kernel(const u16* __restrict__ lar, const u16* __restrict__ aim,
                                                   const u16* __restrict__ cr, const u16* __restrict__ ci,
                                                   const u16* __restrict__ logdt, const u16* __restrict__ xnT,
                                                   u16* __restrict__ yT) {
  __shared__ float Kd[648];
  __shared__ float Kf[2048];
  __shared__ float uf[2048];
  __shared__ float par[4][128];
  const int d = blockIdx.x;
  const float dt = rbf(__expf(0.f) * expf(bf2f(logdt[d])));
  if (threadIdx.x < 128) {
    const int n = threadIdx.x;
    float ea = rbf(expf(bf2f(lar[d * 128 + n])));
    par[0][n] = -ea * dt;
    par[1][n] = bf2f(aim[d * 128 + n]) * dt;
    par[2][n] = bf2f(cr[d * 128 + n]) * dt;
    par[3][n] = bf2f(ci[d * 128 + n]) * dt;
  }
  for (int l = threadIdx.x; l < 2048; l += 256) uf[l] = bf2f(xnT[(size_t)d * 2048 + l]);
  __syncthreads();
  for (int idx = threadIdx.x; idx < 641; idx += 256) {
    const float p = (float)pos_of_idx(idx);
    float acc = 0.f;
    for (int n = 0; n < 128; ++n) {
      float re = par[0][n] * p, im = par[1][n] * p;
      float e = expf(re), s, c;
      sincosf(im, &s, &c);
      acc += par[2][n] * (e * c) - par[3][n] * (e * s);
    }
    Kd[idx] = 2.0f * acc;
  }
  __syncthreads();
  for (int l = threadIdx.x; l < 2048; l += 256) Kf[l] = Kd[idx_of_l(l)];
  __syncthreads();
  for (int l = threadIdx.x; l < 2048; l += 256) {
    float acc = 0.f;
    for (int j = 0; j <= l; ++j) acc += Kf[j] * uf[l - j];
    yT[(size_t)d * 2048 + l] = f2bf(acc);
  }
}

__device__ __forceinline__ bf16x4 tr_read(unsigned addr) {
  bf16x4 r;
  asm volatile("ds_read_b64_tr_b16 %0, %1" : "=v"(r) : "v"(addr) : "memory");
  return r;
}

__device__ __forceinline__ unsigned swz_off(unsigned row, unsigned col) {
  unsigned c32 = col >> 4;
  unsigned f = ((row >> 1) & 1) | (((row >> 3) & 1) << 1);
  return row * 128 + ((c32 ^ f) << 5) + ((col & 15) << 1);
}

__device__ __forceinline__ float gelu_tanh(float x) {
  float k = 0.7978845608028654f * (x + 0.044715f * x * x * x);
  return 0.5f * x * (1.0f + tanhf(k));
}

#define BK 64
__global__ __launch_bounds__(256) void gemm1_kernel(const u16* __restrict__ h, const u16* __restrict__ w1,
                                                    const u16* __restrict__ b1, u16* __restrict__ G) {
  __shared__ __attribute__((aligned(16))) char lds[2][2][BK * 128];
  const int tid = threadIdx.x, wave = tid >> 6, lane = tid & 63;
  const int bm = blockIdx.x & 7, bn = blockIdx.x >> 3;
  const int m0 = bm * 64, n0 = bn * 64;
  const int wm = (wave >> 1) * 32, wn = (wave & 1) * 32;
  f32x4 acc[2][2] = {};
  const int lr = tid >> 3, lc = (tid & 7) * 8;
  bf16x8 ra[2], rb[2];
  auto gload = [&](int k0) {
#pragma unroll
    for (int i = 0; i < 2; ++i) {
      ra[i] = *(const bf16x8*)(h + (size_t)(k0 + lr + 32 * i) * 512 + m0 + lc);
      rb[i] = *(const bf16x8*)(w1 + (size_t)(k0 + lr + 32 * i) * 1792 + n0 + lc);
    }
  };
  auto lstore = [&](int buf) {
#pragma unroll
    for (int i = 0; i < 2; ++i) {
      *(bf16x8*)(lds[buf][0] + swz_off(lr + 32 * i, lc)) = ra[i];
      *(bf16x8*)(lds[buf][1] + swz_off(lr + 32 * i, lc)) = rb[i];
    }
  };
  gload(0);
  lstore(0);
  __syncthreads();
  const int NT = 2048 / BK;
  const unsigned g = lane >> 4, q = (lane & 15) >> 2, p = lane & 3;
  for (int t = 0; t < NT; ++t) {
    const int buf = t & 1;
    if (t + 1 < NT) gload((t + 1) * BK);
    const unsigned baseA = (unsigned)(size_t)lds[buf][0], baseB = (unsigned)(size_t)lds[buf][1];
#pragma unroll
    for (int kk = 0; kk < BK / 32; ++kk) {
      bf16x8 af[2], bfr[2];
#pragma unroll
      for (int i = 0; i < 2; ++i) {
        unsigned r0 = kk * 32 + 8 * g + q;
        bf16x4 a0 = tr_read(baseA + swz_off(r0, wm + 16 * i + 4 * p));
        bf16x4 a1 = tr_read(baseA + swz_off(r0 + 4, wm + 16 * i + 4 * p));
        bf16x4 b0 = tr_read(baseB + swz_off(r0, wn + 16 * i + 4 * p));
        bf16x4 b1v = tr_read(baseB + swz_off(r0 + 4, wn + 16 * i + 4 * p));
        asm volatile("s_waitcnt lgkmcnt(0)" ::: "memory");
        __builtin_amdgcn_sched_barrier(0);
        af[i] = bf16x8{a0[0], a0[1], a0[2], a0[3], a1[0], a1[1], a1[2], a1[3]};
        bfr[i] = bf16x8{b0[0], b0[1], b0[2], b0[3], b1v[0], b1v[1], b1v[2], b1v[3]};
      }
#pragma unroll
      for (int i = 0; i < 2; ++i)
#pragma unroll
        for (int j = 0; j < 2; ++j)
          acc[i][j] = __builtin_amdgcn_mfma_f32_16x16x32_bf16(af[i], bfr[j], acc[i][j], 0, 0, 0);
    }
    if (t + 1 < NT) lstore(buf ^ 1);
    __syncthreads();
  }
#pragma unroll
  for (int i = 0; i < 2; ++i)
#pragma unroll
    for (int j = 0; j < 2; ++j) {
      const int f = n0 + wn + 16 * j + (lane & 15);
      const float bb = bf2f(b1[f]);
#pragma unroll
      for (int r = 0; r < 4; ++r) {
        const int d = m0 + wm + 16 * i + (lane >> 4) * 4 + r;
        float u = rbf(rbf(acc[i][j][r]) + bb);
        G[(size_t)d * 1792 + f] = f2bf(gelu_tanh(u));
      }
    }
}

__device__ __forceinline__ unsigned swzB_off(unsigned row, unsigned ch16) {
  return row * 128 + ((ch16 ^ (row & 7)) << 4);
}
__global__ __launch_bounds__(256) void gemm2_kernel(const u16* __restrict__ w2, const u16* __restrict__ G,
                                                    const u16* __restrict__ b2, const u16* __restrict__ x,
                                                    const u16* __restrict__ xn, const u16* __restrict__ yT,
                                                    const u16* __restrict__ dskip, u16* __restrict__ out) {
  __shared__ __attribute__((aligned(16))) char lds[2][2][BK * 128];
  const int tid = threadIdx.x, wave = tid >> 6, lane = tid & 63;
  const int bn = blockIdx.x & 7, bm = blockIdx.x >> 3;
  const int m0 = bm * 64, n0 = bn * 64;
  const int wm = (wave >> 1) * 32, wn = (wave & 1) * 32;
  f32x4 acc[2][2] = {};
  const int lr = tid >> 3, lc = (tid & 7) * 8;
  bf16x8 ra[2], rb[2];
  auto gload = [&](int k0) {
#pragma unroll
    for (int i = 0; i < 2; ++i) {
      ra[i] = *(const bf16x8*)(w2 + (size_t)(k0 + lr + 32 * i) * 2048 + m0 + lc);
      rb[i] = *(const bf16x8*)(G + (size_t)(n0 + lr + 32 * i) * 1792 + k0 + lc);
    }
  };
  auto lstore = [&](int buf) {
#pragma unroll
    for (int i = 0; i < 2; ++i) {
      *(bf16x8*)(lds[buf][0] + swz_off(lr + 32 * i, lc)) = ra[i];
      *(bf16x8*)(lds[buf][1] + swzB_off(lr + 32 * i, tid & 7)) = rb[i];
    }
  };
  gload(0);
  lstore(0);
  __syncthreads();
  const int NT = 1792 / BK;
  const unsigned g = lane >> 4, q = (lane & 15) >> 2, p = lane & 3;
  for (int t = 0; t < NT; ++t) {
    const int buf = t & 1;
    if (t + 1 < NT) gload((t + 1) * BK);
    const unsigned baseA = (unsigned)(size_t)lds[buf][0];
#pragma unroll
    for (int kk = 0; kk < BK / 32; ++kk) {
      bf16x8 af[2], bfr[2];
#pragma unroll
      for (int i = 0; i < 2; ++i) {
        unsigned r0 = kk * 32 + 8 * g + q;
        bf16x4 a0 = tr_read(baseA + swz_off(r0, wm + 16 * i + 4 * p));
        bf16x4 a1 = tr_read(baseA + swz_off(r0 + 4, wm + 16 * i + 4 * p));
        asm volatile("s_waitcnt lgkmcnt(0)" ::: "memory");
        __builtin_amdgcn_sched_barrier(0);
        af[i] = bf16x8{a0[0], a0[1], a0[2], a0[3], a1[0], a1[1], a1[2], a1[3]};
        bfr[i] = *(const bf16x8*)(lds[buf][1] + swzB_off(wn + 16 * i + (lane & 15), kk * 4 + (lane >> 4)));
      }
#pragma unroll
      for (int i = 0; i < 2; ++i)
#pragma unroll
        for (int j = 0; j < 2; ++j)
          acc[i][j] = __builtin_amdgcn_mfma_f32_16x16x32_bf16(af[i], bfr[j], acc[i][j], 0, 0, 0);
    }
    if (t + 1 < NT) lstore(buf ^ 1);
    __syncthreads();
  }
#pragma unroll
  for (int i = 0; i < 2; ++i)
#pragma unroll
    for (int j = 0; j < 2; ++j) {
      const int d = n0 + wn + 16 * j + (lane & 15);
      const float sk = bf2f(dskip[d]);
#pragma unroll
      for (int r = 0; r < 4; ++r) {
        const int s = m0 + wm + 16 * i + (lane >> 4) * 4 + r;
        float val = rbf(rbf(acc[i][j][r]) + bf2f(b2[s]));
        float a = rbf(bf2f(yT[(size_t)d * 2048 + s]) + rbf(bf2f(xn[(size_t)s * 512 + d]) * sk));
        float o = rbf(rbf(bf2f(x[(size_t)s * 512 + d]) + a) + val);
        out[(size_t)s * 512 + d] = f2bf(o);
      }
    }
}

extern "C" void kernel_launch(void* const* d_in, const int* in_sizes, int n_in,
                              void* d_out, int out_size, void* d_ws, size_t ws_size,
                              hipStream_t stream) {
  const u16* x = (const u16*)d_in[0];
  const u16* lar = (const u16*)d_in[1];
  const u16* aim = (const u16*)d_in[2];
  const u16* cr = (const u16*)d_in[3];
  const u16* ci = (const u16*)d_in[4];
  const u16* logdt = (const u16*)d_in[5];
  const u16* dskip = (const u16*)d_in[6];
  const u16* w1 = (const u16*)d_in[8];
  const u16* b1 = (const u16*)d_in[9];
  const u16* w2 = (const u16*)d_in[10];
  const u16* b2 = (const u16*)d_in[11];
  u16* out = (u16*)d_out;
  char* ws = (char*)d_ws;
  const size_t SZ = (size_t)2048 * 512 * 2;
  u16* xn = (u16*)(ws);
  u16* h = (u16*)(ws + SZ);
  u16* xnT = (u16*)(ws + 2 * SZ);
  u16* yT = (u16*)(ws + 3 * SZ);
  u16* G = (u16*)(ws + 4 * SZ);
  ln_kernel<<<64, 256, 0, stream>>>(x, xn, h, xnT);
  conv_kernel<<<512, 256, 0, stream>>>(lar, aim, cr, ci, logdt, xnT, yT);
  gemm1_kernel<<<8 * 28, 256, 0, stream>>>(h, w1, b1, G);
  gemm2_kernel<<<32 * 8, 256, 0, stream>>>(w2, G, b2, x, xn, yT, dskip, out);
}
```

```cpp
#include <hip/hip_runtime.h>
#include <hip/hip_bf16.h>
#include <stdint.h>

typedef __attribute__((ext_vector_type(8))) short bf16x8;
typedef __attribute__((ext_vector_type(4))) short bf16x4;
typedef __attribute__((ext_vector_type(4))) float f32x4;
typedef unsigned short u16;

#define L_SEQ 2048
#define D_CH 512
#define N_ST 128
#define F_FF 1792

__device__ __forceinline__ float bf2f(u16 v) { return __uint_as_float(((unsigned)v) << 16); }
__device__ __forceinline__ u16 f2bf(float f) {
  unsigned u = __float_as_uint(f);
  u += 0x7fffu + ((u >> 16) & 1u);
  return (u16)(u >> 16);
}
__device__ __forceinline__ float rbf(float f) { return bf2f(f2bf(f)); }

__device__ __forceinline__ float wave_sum(float v) {
#pragma unroll
  for (int o = 32; o > 0; o >>= 1) v += __shfl_xor(v, o, 64);
  return v;
}

__device__ __forceinline__ void ln8(const float* x, float* y) {
  float s = 0.f;
#pragma unroll
  for (int i = 0; i < 8; ++i) s += x[i];
  s = wave_sum(s);
  float mean = s * (1.0f / 512.0f);
  float q = 0.f;
#pragma unroll
  for (int i = 0; i < 8; ++i) { float d = x[i] - mean; q += d * d; }
  q = wave_sum(q);
  float var = q * (1.0f / 512.0f);
  float m_b = rbf(mean);
  float v_b = rbf(var);
  float den = rbf(sqrtf(rbf(v_b + 1e-5f)));
#pragma unroll
  for (int i = 0; i < 8; ++i) y[i] = rbf(rbf(x[i] - m_b) / den);
}

__global__ __launch_bounds__(256) void ln_kernel(const u16* __restrict__ x, u16* __restrict__ xn,
                                                 u16* __restrict__ h, u16* __restrict__ xnT) {
  __shared__ u16 tr[32][512 + 8];
  const int wave = threadIdx.x >> 6, lane = threadIdx.x & 63;
  const int s0 = blockIdx.x * 32;
  for (int rr = 0; rr < 8; ++rr) {
    const int r = wave * 8 + rr;
    const int s = s0 + r;
    bf16x8 v = *(const bf16x8*)(x + (size_t)s * 512 + lane * 8);
    float xf[8], a[8], b[8];
#pragma unroll
    for (int i = 0; i < 8; ++i) xf[i] = bf2f((u16)v[i]);
    ln8(xf, a);
    ln8(a, b);
    bf16x8 oa, ob;
#pragma unroll
    for (int i = 0; i < 8; ++i) { oa[i] = (short)f2bf(a[i]); ob[i] = (short)f2bf(b[i]); }
    *(bf16x8*)(xn + (size_t)s * 512 + lane * 8) = oa;
    *(bf16x8*)(h + (size_t)s * 512 + lane * 8) = ob;
#pragma unroll
    for (int i = 0; i < 8; ++i) tr[r][lane * 8 + i] = (u16)oa[i];
  }
  __syncthreads();
  for (int idx = threadIdx.x; idx < 512 * 4; idx += 256) {
    const int d = idx >> 2, c = idx & 3;
    bf16x8 o;
#pragma unroll
    for (int i = 0; i < 8; ++i) o[i] = (short)tr[c * 8 + i][d];
    *(bf16x8*)(xnT + (size_t)d * 2048 + s0 + c * 8) = o;
  }
}

__device__ __forceinline__ int pos_of_idx(int idx) {
  if (idx < 256) return idx;
  if (idx < 384) return 256 + 2 * (idx - 256);
  if (idx < 512) return 512 + 4 * (idx - 384);
  return 1024 + 8 * (idx - 512);
}
__device__ __forceinline__ int idx_of_l(int l) {
  int p = (int)rbf((float)l);
  if (p < 256) return p;
  if (p < 512) return 256 + ((p - 256) >> 1);
  if (p < 1024) return 384 + ((p - 512) >> 2);
  return 512 + ((p - 1024) >> 3);
}

typedef __attribute__((ext_vector_type(16))) float f32x16;

__device__ __forceinline__ float2 cexp_acc(float re, float im) {
  float e = expf(re), s, c;
  sincosf(im, &s, &c);
  return make_float2(e * c, e * s);
}
__device__ __forceinline__ float2 cmul(float2 a, float2 b) {
  return make_float2(a.x * b.x - a.y * b.y, a.x * b.y + a.y * b.x);
}

#define CP_STRIDE 2192
__global__ __launch_bounds__(256) void conv_kernel(const u16* __restrict__ lar, const u16* __restrict__ aim,
                                                   const u16* __restrict__ cr, const u16* __restrict__ ci,
                                                   const u16* __restrict__ logdt, const u16* __restrict__ xnT,
                                                   u16* __restrict__ yT) {
  __shared__ __attribute__((aligned(16))) char smem[32768 + 4 * 2112];
  __shared__ float Kd[648];
  __shared__ __attribute__((aligned(16))) u16 Khi[96 * 40];
  __shared__ __attribute__((aligned(16))) u16 Klo[96 * 40];
  __shared__ __attribute__((aligned(16))) u16 cp[8 * CP_STRIDE + 64];
  float2(*tabA)[16] = (float2(*)[16])smem;
  float2(*tabB)[16] = (float2(*)[16])(smem + 16384);
  const int d = blockIdx.x, tid = threadIdx.x, wave = tid >> 6, lane = tid & 63;
  const float dt = rbf(expf(bf2f(logdt[d])));
  const int n = tid & 127;
  const float ar = -rbf(expf(bf2f(lar[d * 128 + n]))) * dt;
  const float ai = bf2f(aim[d * 128 + n]) * dt;
  const float2 Cc = make_float2(bf2f(cr[d * 128 + n]) * dt, bf2f(ci[d * 128 + n]) * dt);

  for (int idx = tid; idx < 8 * CP_STRIDE; idx += 256) {
    const int k = idx / CP_STRIDE, xx = idx - k * CP_STRIDE;
    const int ui = 2048 - xx + k;
    u16 v = 0;
    if (ui >= 0 && ui < 2048) v = xnT[(size_t)d * 2048 + ui];
    cp[idx] = v;
  }
  for (int R = 0; R < 4; ++R) {
    const float bstep = (float)(1 << R);
    const float astep = (float)(16 << R);
    const float base = (R == 0) ? 0.f : (float)(128 << R);
    const int numA = (R == 0) ? 16 : (R == 3 ? 9 : 8);
    __syncthreads();
    if (tid < 128) {
      float2 e = cexp_acc(ar * bstep, ai * bstep);
      float2 p = make_float2(1.f, 0.f);
      for (int b = 0; b < 16; ++b) { tabB[n][b] = p; p = cmul(p, e); }
    } else {
      float2 e = cexp_acc(ar * astep, ai * astep);
      float2 p = cmul(Cc, cexp_acc(ar * base, ai * base));
      for (int a = 0; a < numA; ++a) { tabA[n][a] = p; p = cmul(p, e); }
    }
    __syncthreads();
    const int nout = (R == 0) ? 256 : (R == 3 ? 129 : 128);
    if (tid < nout) {
      const int a = tid >> 4, b = tid & 15;
      float acc = 0.f;
#pragma unroll 8
      for (int nn = 0; nn < 128; ++nn) {
        float2 x = tabA[nn][a], y = tabB[nn][b];
        acc += x.x * y.x - x.y * y.y;
      }
      const int off = (R == 0) ? 0 : 128 + 128 * R;
      Kd[off + tid] = 2.0f * acc;
    }
  }
  __syncthreads();
  for (int idx = tid; idx < 96 * 32; idx += 256) {
    const int c = (idx >> 5) - 32, e = idx & 31;
    float kv = 0.f;
    if (c >= 0) kv = Kd[idx_of_l(c * 32 + e)];
    u16 hi = f2bf(kv);
    u16 lo = f2bf(kv - bf2f(hi));
    Khi[(idx >> 5) * 40 + e] = hi;
    Klo[(idx >> 5) * 40 + e] = lo;
  }
  __syncthreads();
  f32x16 acc0 = {}, acc1 = {};
  {
    const int r = lane & 31, hh = lane >> 5;
    const u16* abase = cp + (r & 7) * CP_STRIDE + 2048 - 8 * (r >> 3) + 8 * hh;
    const int i = lane & 31;
    for (int m = wave; m < 64; m += 4) {
      bf16x8 a0 = *(const bf16x8*)(abase - 32 * m);
      bf16x8 a1 = *(const bf16x8*)(abase - 32 * m + 16);
      {
        const int ch = (32 + i - m + 32) * 40 + 8 * hh;
        bf16x8 bh0 = *(const bf16x8*)(Khi + ch), bh1 = *(const bf16x8*)(Khi + ch + 16);
        bf16x8 bl0 = *(const bf16x8*)(Klo + ch), bl1 = *(const bf16x8*)(Klo + ch + 16);
        acc1 = __builtin_amdgcn_mfma_f32_32x32x16_bf16(a0, bh0, acc1, 0, 0, 0);
        acc1 = __builtin_amdgcn_mfma_f32_32x32x16_bf16(a1, bh1, acc1, 0, 0, 0);
        acc1 = __builtin_amdgcn_mfma_f32_32x32x16_bf16(a0, bl0, acc1, 0, 0, 0);
        acc1 = __builtin_amdgcn_mfma_f32_32x32x16_bf16(a1, bl1, acc1, 0, 0, 0);
      }
      if (m < 32) {
        const int ch = (i - m + 32) * 40 + 8 * hh;
        bf16x8 bh0 = *(const bf16x8*)(Khi + ch), bh1 = *(const bf16x8*)(Khi + ch + 16);
        bf16x8 bl0 = *(const bf16x8*)(Klo + ch), bl1 = *(const bf16x8*)(Klo + ch + 16);
        acc0 = __builtin_amdgcn_mfma_f32_32x32x16_bf16(a0, bh0, acc0, 0, 0, 0);
        acc0 = __builtin_amdgcn_mfma_f32_32x32x16_bf16(a1, bh1, acc0, 0, 0, 0);
        acc0 = __builtin_amdgcn_mfma_f32_32x32x16_bf16(a0, bl0, acc0, 0, 0, 0);
        acc0 = __builtin_amdgcn_mfma_f32_32x32x16_bf16(a1, bl1, acc0, 0, 0, 0);
      }
    }
  }
  float* red = (float*)smem;
  {
    const int i = lane & 31, hh = lane >> 5;
#pragma unroll
    for (int rg = 0; rg < 16; ++rg) {
      const int row = (rg & 3) + 8 * (rg >> 2) + 4 * hh;
      red[wave * 2112 + i * 33 + row] = acc0[rg];
      red[wave * 2112 + (32 + i) * 33 + row] = acc1[rg];
    }
  }
  __syncthreads();
  for (int l = tid; l < 2048; l += 256) {
    const int o = (l >> 5) * 33 + (l & 31);
    float v = (red[o] + red[2112 + o]) + (red[2 * 2112 + o] + red[3 * 2112 + o]);
    yT[(size_t)d * 2048 + l] = f2bf(v);
  }
}

__device__ __forceinline__ bf16x4 tr_read(unsigned addr) {
  bf16x4 r;
  asm volatile("ds_read_b64_tr_b16 %0, %1" : "=v"(r) : "v"(addr) : "memory");
  return r;
}

__device__ __forceinline__ unsigned swz_off(unsigned row, unsigned col) {
  unsigned c32 = col >> 4;
  unsigned f = ((row >> 1) & 1) | (((row >> 3) & 1) << 1);
  return row * 128 + ((c32 ^ f) << 5) + ((col & 15) << 1);
}

__device__ __forceinline__ float gelu_tanh(float x) {
  float k = 0.7978845608028654f * (x + 0.044715f * x * x * x);
  return 0.5f * x * (1.0f + tanhf(k));
}

#define BK 64
__global__ __launch_bounds__(256) void gemm1_kernel(const u16* __restrict__ h, const u16* __restrict__ w1,
                                                    const u16* __restrict__ b1, u16* __restrict__ G) {
  __shared__ __attribute__((aligned(16))) char lds[2][2][BK * 128];
  const int tid = threadIdx.x, wave = tid >> 6, lane = tid & 63;
  const int bm = blockIdx.x & 7, bn = blockIdx.x >> 3;
  const int m0 = bm * 64, n0 = bn * 64;
  const int wm = (wave >> 1) * 32, wn = (wave & 1) * 32;
  f32x4 acc[2][2] = {};
  const int lr = tid >> 3, lc = (tid & 7) * 8;
  bf16x8 ra[2], rb[2];
  auto gload = [&](int k0) {
#pragma unroll
    for (int i = 0; i < 2; ++i) {
      ra[i] = *(const bf16x8*)(h + (size_t)(k0 + lr + 32 * i) * 512 + m0 + lc);
      rb[i] = *(const bf16x8*)(w1 + (size_t)(k0 + lr + 32 * i) * 1792 + n0 + lc);
    }
  };
  auto lstore = [&](int buf) {
#pragma unroll
    for (int i = 0; i < 2; ++i) {
      *(bf16x8*)(lds[buf][0] + swz_off(lr + 32 * i, lc)) = ra[i];
      *(bf16x8*)(lds[buf][1] + swz_off(lr + 32 * i, lc)) = rb[i];
    }
  };
  gload(0);
  lstore(0);
  __syncthreads();
  const int NT = 2048 / BK;
  const unsigned g = lane >> 4, q = (lane & 15) >> 2, p = lane & 3;
  for (int t = 0; t < NT; ++t) {
    const int buf = t & 1;
    if (t + 1 < NT) gload((t + 1) * BK);
    const unsigned baseA = (unsigned)(size_t)lds[buf][0], baseB = (unsigned)(size_t)lds[buf][1];
#pragma unroll
    for (int kk = 0; kk < BK / 32; ++kk) {
      bf16x8 af[2], bfr[2];
#pragma unroll
      for (int i = 0; i < 2; ++i) {
        unsigned r0 = kk * 32 + 8 * g + q;
        bf16x4 a0 = tr_read(baseA + swz_off(r0, wm + 16 * i + 4 * p));
        bf16x4 a1 = tr_read(baseA + swz_off(r0 + 4, wm + 16 * i + 4 * p));
        bf16x4 b0 = tr_read(baseB + swz_off(r0, wn + 16 * i + 4 * p));
        bf16x4 b1v = tr_read(baseB + swz_off(r0 + 4, wn + 16 * i + 4 * p));
        asm volatile("s_waitcnt lgkmcnt(0)" ::: "memory");
        __builtin_amdgcn_sched_barrier(0);
        af[i] = bf16x8{a0[0], a0[1], a0[2], a0[3], a1[0], a1[1], a1[2], a1[3]};
        bfr[i] = bf16x8{b0[0], b0[1], b0[2], b0[3], b1v[0], b1v[1], b1v[2], b1v[3]};
      }
#pragma unroll
      for (int i = 0; i < 2; ++i)
#pragma unroll
        for (int j = 0; j < 2; ++j)
          acc[i][j] = __builtin_amdgcn_mfma_f32_16x16x32_bf16(af[i], bfr[j], acc[i][j], 0, 0, 0);
    }
    if (t + 1 < NT) lstore(buf ^ 1);
    __syncthreads();
  }
#pragma unroll
  for (int i = 0; i < 2; ++i)
#pragma unroll
    for (int j = 0; j < 2; ++j) {
      const int f = n0 + wn + 16 * j + (lane & 15);
      const float bb = bf2f(b1[f]);
#pragma unroll
      for (int r = 0; r < 4; ++r) {
        const int d = m0 + wm + 16 * i + (lane >> 4) * 4 + r;
        float u = rbf(rbf(acc[i][j][r]) + bb);
        G[(size_t)d * 1792 + f] = f2bf(gelu_tanh(u));
      }
    }
}

__device__ __forceinline__ unsigned swzB_off(unsigned row, unsigned ch16) {
  return row * 128 + ((ch16 ^ (row & 7)) << 4);
}
__global__ __launch_bounds__(256) void gemm2_kernel(const u16* __restrict__ w2, const u16* __restrict__ G,
                                                    const u16* __restrict__ b2, const u16* __restrict__ x,
                                                    const u16* __restrict__ xn, const u16* __restrict__ yT,
                                                    const u16* __restrict__ dskip, u16* __restrict__ out) {
  __shared__ __attribute__((aligned(16))) char lds[2][2][BK * 128];
  const int tid = threadIdx.x, wave = tid >> 6, lane = tid & 63;
  const int bn = blockIdx.x & 7, bm = blockIdx.x >> 3;
  const int m0 = bm * 64, n0 = bn * 64;
  const int wm = (wave >> 1) * 32, wn = (wave & 1) * 32;
  f32x4 acc[2][2] = {};
  const int lr = tid >> 3, lc = (tid & 7) * 8;
  bf16x8 ra[2], rb[2];
  auto gload = [&](int k0) {
#pragma unroll
    for (int i = 0; i < 2; ++i) {
      ra[i] = *(const bf16x8*)(w2 + (size_t)(k0 + lr + 32 * i) * 2048 + m0 + lc);
      rb[i] = *(const bf16x8*)(G + (size_t)(n0 + lr + 32 * i) * 1792 + k0 + lc);
    }
  };
  auto lstore = [&](int buf) {
#pragma unroll
    for (int i = 0; i < 2; ++i) {
      *(bf16x8*)(lds[buf][0] + swz_off(lr + 32 * i, lc)) = ra[i];
      *(bf16x8*)(lds[buf][1] + swzB_off(lr + 32 * i, tid & 7)) = rb[i];
    }
  };
  gload(0);
  lstore(0);
  __syncthreads();
  const int NT = 1792 / BK;
  const unsigned g = lane >> 4, q = (lane & 15) >> 2, p = lane & 3;
  for (int t = 0; t < NT; ++t) {
    const int buf = t & 1;
    if (t + 1 < NT) gload((t + 1) * BK);
    const unsigned baseA = (unsigned)(size_t)lds[buf][0];
#pragma unroll
    for (int kk = 0; kk < BK / 32; ++kk) {
      bf16x8 af[2], bfr[2];
#pragma unroll
      for (int i = 0; i < 2; ++i) {
        unsigned r0 = kk * 32 + 8 * g + q;
        bf16x4 a0 = tr_read(baseA + swz_off(r0, wm + 16 * i + 4 * p));
        bf16x4 a1 = tr_read(baseA + swz_off(r0 + 4, wm + 16 * i + 4 * p));
        asm volatile("s_waitcnt lgkmcnt(0)" ::: "memory");
        __builtin_amdgcn_sched_barrier(0);
        af[i] = bf16x8{a0[0], a0[1], a0[2], a0[3], a1[0], a1[1], a1[2], a1[3]};
        bfr[i] = *(const bf16x8*)(lds[buf][1] + swzB_off(wn + 16 * i + (lane & 15), kk * 4 + (lane >> 4)));
      }
#pragma unroll
      for (int i = 0; i < 2; ++i)
#pragma unroll
        for (int j = 0; j < 2; ++j)
          acc[i][j] = __builtin_amdgcn_mfma_f32_16x16x32_bf16(af[i], bfr[j], acc[i][j], 0, 0, 0);
    }
    if (t + 1 < NT) lstore(buf ^ 1);
    __syncthreads();
  }
#pragma unroll
  for (int i = 0; i < 2; ++i)
#pragma unroll
    for (int j = 0; j < 2; ++j) {
      const int d = n0 + wn + 16 * j + (lane & 15);
      const float sk = bf2f(dskip[d]);
#pragma unroll
      for (int r = 0; r < 4; ++r) {
        const int s = m0 + wm + 16 * i + (lane >> 4) * 4 + r;
        float val = rbf(rbf(acc[i][j][r]) + bf2f(b2[s]));
        float a = rbf(bf2f(yT[(size_t)d * 2048 + s]) + rbf(bf2f(xn[(size_t)s * 512 + d]) * sk));
        float o = rbf(rbf(bf2f(x[(size_t)s * 512 + d]) + a) + val);
        out[(size_t)s * 512 + d] = f2bf(o);
      }
    }
}

extern "C" void kernel_launch(void* const* d_in, const int* in_sizes, int n_in,
                              void* d_out, int out_size, void* d_ws, size_t ws_size,
                              hipStream_t stream) {
  const u16* x = (const u16*)d_in[0];
  const u16* lar = (const u16*)d_in[1];
  const u16* aim = (const u16*)d_in[2];
  const u16* cr = (const u16*)d_in[3];
  const u16* ci = (const u16*)d_in[4];
  const u16* logdt = (const u16*)d_in[5];
  const u16* dskip = (const u16*)d_in[6];
  const u16* w1 = (const u16*)d_in[8];
  const u16* b1 = (const u16*)d_in[9];
  const u16* w2 = (const u16*)d_in[10];
  const u16* b2 = (const u16*)d_in[11];
  u16* out = (u16*)d_out;
  char* ws = (char*)d_ws;
  const size_t SZ = (size_t)2048 * 512 * 2;
  u16* xn = (u16*)(ws);
  u16* h = (u16*)(ws + SZ);
  u16* xnT = (u16*)(ws + 2 * SZ);
  u16* yT = (u16*)(ws + 3 * SZ);
  u16* G = (u16*)(ws + 4 * SZ);
  ln_kernel<<<64, 256, 0, stream>>>(x, xn, h, xnT);
  conv_kernel<<<512, 256, 0, stream>>>(lar, aim, cr, ci, logdt, xnT, yT);
  gemm1_kernel<<<8 * 28, 256, 0, stream>>>(h, w1, b1, G);
  gemm2_kernel<<<32 * 8, 256, 0, stream>>>(w2, G, b2, x, xn, yT, dskip, out);
}
```

```cpp
#include <hip/hip_runtime.h>
#include <hip/hip_bf16.h>
#include <stdint.h>

typedef __attribute__((ext_vector_type(8))) short bf16x8;
typedef __attribute__((ext_vector_type(4))) short bf16x4;
typedef __attribute__((ext_vector_type(4))) float f32x4;
typedef __attribute__((ext_vector_type(16))) float f32x16;
typedef unsigned short u16;

__device__ __forceinline__ float bf2f(u16 v) { return __uint_as_float(((unsigned)v) << 16); }
__device__ __forceinline__ u16 f2bf(float f) {
  unsigned u = __float_as_uint(f);
  u += 0x7fffu + ((u >> 16) & 1u);
  return (u16)(u >> 16);
}
__device__ __forceinline__ float rbf(float f) { return bf2f(f2bf(f)); }

__device__ __forceinline__ float wave_sum(float v) {
#pragma unroll
  for (int o = 32; o > 0; o >>= 1) v += __shfl_xor(v, o, 64);
  return v;
}

__device__ __forceinline__ void ln8(const float* x, float* y) {
  float s = 0.f;
#pragma unroll
  for (int i = 0; i < 8; ++i) s += x[i];
  s = wave_sum(s);
  float mean = s * (1.0f / 512.0f);
  float q = 0.f;
#pragma unroll
  for (int i = 0; i < 8; ++i) { float d = x[i] - mean; q += d * d; }
  q = wave_sum(q);
  float var = q * (1.0f / 512.0f);
  float m_b = rbf(mean);
  float v_b = rbf(var);
  float den = rbf(sqrtf(rbf(v_b + 1e-5f)));
#pragma unroll
  for (int i = 0; i < 8; ++i) y[i] = rbf(rbf(x[i] - m_b) / den);
}

__device__ __forceinline__ int idx_of_l(int l) {
  int p = (int)rbf((float)l);
  if (p < 256) return p;
  if (p < 512) return 256 + ((p - 256) >> 1);
  if (p < 1024) return 384 + ((p - 512) >> 2);
  return 512 + ((p - 1024) >> 3);
}
__device__ __forceinline__ float2 cexp_acc(float re, float im) {
  float e = expf(re), s, c;
  sincosf(im, &s, &c);
  return make_float2(e * c, e * s);
}
__device__ __forceinline__ float2 cmul(float2 a, float2 b) {
  return make_float2(a.x * b.x - a.y * b.y, a.x * b.y + a.y * b.x);
}

__global__ __launch_bounds__(256) void prep_kernel(const u16* __restrict__ x, u16* __restrict__ xn,
                                                   u16* __restrict__ h, u16* __restrict__ xnT,
                                                   const u16* __restrict__ lar, const u16* __restrict__ aim,
                                                   const u16* __restrict__ cr, const u16* __restrict__ ci,
                                                   const u16* __restrict__ logdt, u16* __restrict__ Khi,
                                                   u16* __restrict__ Klo) {
  __shared__ __attribute__((aligned(16))) char smem[57344 + 2816];
  const int tid = threadIdx.x, wave = tid >> 6, lane = tid & 63;
  if (blockIdx.x < 128) {
    u16(*tr)[512 + 8] = (u16(*)[512 + 8])smem;
    const int s0 = blockIdx.x * 16;
    bf16x8 v[4];
#pragma unroll
    for (int rr = 0; rr < 4; ++rr) v[rr] = *(const bf16x8*)(x + (size_t)(s0 + wave * 4 + rr) * 512 + lane * 8);
#pragma unroll
    for (int rr = 0; rr < 4; ++rr) {
      const int r = wave * 4 + rr;
      const int s = s0 + r;
      float xf[8], a[8], b[8];
#pragma unroll
      for (int i = 0; i < 8; ++i) xf[i] = bf2f((u16)v[rr][i]);
      ln8(xf, a);
      ln8(a, b);
      bf16x8 oa, ob;
#pragma unroll
      for (int i = 0; i < 8; ++i) { oa[i] = (short)f2bf(a[i]); ob[i] = (short)f2bf(b[i]); }
      *(bf16x8*)(xn + (size_t)s * 512 + lane * 8) = oa;
      *(bf16x8*)(h + (size_t)s * 512 + lane * 8) = ob;
#pragma unroll
      for (int i = 0; i < 8; ++i) tr[r][lane * 8 + i] = (u16)oa[i];
    }
    __syncthreads();
    for (int idx = tid; idx < 512 * 2; idx += 256) {
      const int d = idx >> 1, c = idx & 1;
      bf16x8 o;
#pragma unroll
      for (int i = 0; i < 8; ++i) o[i] = (short)tr[c * 8 + i][d];
      *(bf16x8*)(xnT + (size_t)d * 2048 + s0 + c * 8) = o;
    }
    return;
  }
  const int d = blockIdx.x - 128;
  float2(*tabA)[24] = (float2(*)[24])smem;
  float2(*tabB)[32] = (float2(*)[32])(smem + 24576);
  float* Kd = (float*)(smem + 57344);
  const float dt = rbf(expf(bf2f(logdt[d])));
  const int n = tid & 127, half = tid >> 7;
  const float ar = -rbf(expf(bf2f(lar[d * 128 + n]))) * dt;
  const float ai = bf2f(aim[d * 128 + n]) * dt;
  const float2 Cc = make_float2(bf2f(cr[d * 128 + n]) * dt, bf2f(ci[d * 128 + n]) * dt);
  for (int pass = 0; pass < 2; ++pass) {
    const int R = pass * 2 + half;
    {
      const float bstep = (float)(1 << R);
      const float astep = (float)(16 << R);
      const float base = (R == 0) ? 0.f : (float)(128 << R);
      const int numA = (R == 0) ? 16 : (R == 3 ? 8 : 8);
      float2 e = cexp_acc(ar * bstep, ai * bstep);
      float2 p = make_float2(1.f, 0.f);
      for (int b = 0; b < 16; ++b) { tabB[n][half * 16 + b] = p; p = cmul(p, e); }
      e = cexp_acc(ar * astep, ai * astep);
      p = cmul(Cc, cexp_acc(ar * base, ai * base));
      for (int a = 0; a < numA; ++a) { tabA[n][half * 16 + a] = p; p = cmul(p, e); }
    }
    __syncthreads();
    if (pass == 0) {
      {
        const int a = tid >> 4, b = tid & 15;
        float acc = 0.f;
#pragma unroll 8
        for (int nn = 0; nn < 128; ++nn) { float2 xx = tabA[nn][a], yy = tabB[nn][b]; acc += xx.x * yy.x - xx.y * yy.y; }
        Kd[tid] = 2.0f * acc;
      }
      if (tid < 128) {
        const int a = tid >> 4, b = tid & 15;
        float acc = 0.f;
#pragma unroll 8
        for (int nn = 0; nn < 128; ++nn) { float2 xx = tabA[nn][16 + a], yy = tabB[nn][16 + b]; acc += xx.x * yy.x - xx.y * yy.y; }
        Kd[256 + tid] = 2.0f * acc;
      }
    } else {
      const int t = tid & 127;
      const int a = t >> 4, b = t & 15;
      float acc = 0.f;
#pragma unroll 8
      for (int nn = 0; nn < 128; ++nn) { float2 xx = tabA[nn][half * 16 + a], yy = tabB[nn][half * 16 + b]; acc += xx.x * yy.x - xx.y * yy.y; }
      Kd[384 + tid] = 2.0f * acc;
    }
    __syncthreads();
  }
  {
    float v = 0.f;
    if (tid < 128) { float2 p = cmul(Cc, cexp_acc(ar * 2048.f, ai * 2048.f)); v = p.x; }
    float* part = (float*)smem;
    if (tid < 128) part[tid] = v;
    __syncthreads();
    if (wave == 0) {
      float s = part[lane] + part[lane + 64];
      s = wave_sum(s);
      if (lane == 0) Kd[640] = 2.0f * s;
    }
    __syncthreads();
  }
  {
    bf16x8 hi, lo;
#pragma unroll
    for (int e = 0; e < 8; ++e) {
      float kv = Kd[idx_of_l(tid * 8 + e)];
      u16 hv = f2bf(kv);
      hi[e] = (short)hv;
      lo[e] = (short)f2bf(kv - bf2f(hv));
    }
    *(bf16x8*)(Khi + (size_t)d * 2048 + tid * 8) = hi;
    *(bf16x8*)(Klo + (size_t)d * 2048 + tid * 8) = lo;
  }
}

__device__ __forceinline__ unsigned kslow_src_chunk(unsigned row, unsigned c16) {
  unsigned f = ((row >> 1) & 1) | (((row >> 3) & 1) << 1);
  return ((((c16 >> 1) ^ f)) << 1) | (c16 & 1);
}
__device__ __forceinline__ unsigned swz_off(unsigned row, unsigned col) {
  unsigned c32 = col >> 4;
  unsigned f = ((row >> 1) & 1) | (((row >> 3) & 1) << 1);
  return row * 128 + ((c32 ^ f) << 5) + ((col & 15) << 1);
}
__device__ __forceinline__ float gelu_tanh(float x) {
  float k = 0.7978845608028654f * (x + 0.044715f * x * x * x);
  return 0.5f * x * (1.0f + tanhf(k));
}
#define GLDS(src, dst) __builtin_amdgcn_global_load_lds((const unsigned*)(src), (unsigned*)(dst), 16, 0, 0)
#define NS 6
#define STAGE_BYTES 16384
#define WAIT_VM(n) asm volatile("s_waitcnt vmcnt(" #n ")" ::: "memory")

__device__ __forceinline__ void tr_read8(unsigned a0, unsigned a1, unsigned a2, unsigned a3,
                                         bf16x4& r0, bf16x4& r1, bf16x4& r2, bf16x4& r3) {
  asm volatile("ds_read_b64_tr_b16 %0, %4\n\tds_read_b64_tr_b16 %1, %5\n\tds_read_b64_tr_b16 %2, %6\n\tds_read_b64_tr_b16 %3, %7"
               : "=&v"(r0), "=&v"(r1), "=&v"(r2), "=&v"(r3) : "v"(a0), "v"(a1), "v"(a2), "v"(a3) : "memory");
}

#define CP_STRIDE 2192

__global__ __launch_bounds__(256) void mid_kernel(const u16* __restrict__ h, const u16* __restrict__ w1,
                                                  const u16* __restrict__ b1, u16* __restrict__ G,
                                                  const u16* __restrict__ xnT, const u16* __restrict__ Khg,
                                                  const u16* __restrict__ Klg, u16* __restrict__ yT) {
  __shared__ __attribute__((aligned(16))) char smem[NS * STAGE_BYTES];
  const int tid = threadIdx.x, wave = tid >> 6, lane = tid & 63;
  if (blockIdx.x < 224) {
    const int bm = blockIdx.x & 7, bn = blockIdx.x >> 3;
    const int m0 = bm * 64, n0 = bn * 64;
    const int wm = (wave >> 1) * 32, wn = (wave & 1) * 32;
    f32x4 acc[2][2] = {};
    const u16* srcA[2]; const u16* srcB[2];
#pragma unroll
    for (int i = 0; i < 2; ++i) {
      const unsigned row = wave * 16 + i * 8 + (lane >> 3);
      const unsigned sc = kslow_src_chunk(row, lane & 7);
      srcA[i] = h + (size_t)row * 512 + m0 + sc * 8;
      srcB[i] = w1 + (size_t)row * 1792 + n0 + sc * 8;
    }
    auto issue = [&](int t) {
      char* st = smem + (t % NS) * STAGE_BYTES;
#pragma unroll
      for (int i = 0; i < 2; ++i) {
        GLDS(srcA[i] + (size_t)t * 64 * 512, st + (wave * 16 + i * 8) * 128);
        GLDS(srcB[i] + (size_t)t * 64 * 1792, st + 8192 + (wave * 16 + i * 8) * 128);
      }
    };
    const int NT = 32;
#pragma unroll
    for (int s = 0; s < NS - 1; ++s) issue(s);
    const unsigned g = lane >> 4, q = (lane & 15) >> 2, p = lane & 3;
    for (int t = 0; t < NT; ++t) {
      if (t < NT - (NS - 1)) WAIT_VM(16); else WAIT_VM(0);
      __builtin_amdgcn_s_barrier();
      if (t + NS - 1 < NT) issue(t + NS - 1);
      const unsigned baseA = (unsigned)(size_t)(smem + (t % NS) * STAGE_BYTES), baseB = baseA + 8192;
#pragma unroll
      for (int kk = 0; kk < 2; ++kk) {
        const unsigned r0 = kk * 32 + 8 * g + q;
        bf16x4 a00, a01, a10, a11, b00, b01, b10, b11;
        tr_read8(baseA + swz_off(r0, wm + 4 * p), baseA + swz_off(r0 + 4, wm + 4 * p),
                 baseA + swz_off(r0, wm + 16 + 4 * p), baseA + swz_off(r0 + 4, wm + 16 + 4 * p), a00, a01, a10, a11);
        tr_read8(baseB + swz_off(r0, wn + 4 * p), baseB + swz_off(r0 + 4, wn + 4 * p),
                 baseB + swz_off(r0, wn + 16 + 4 * p), baseB + swz_off(r0 + 4, wn + 16 + 4 * p), b00, b01, b10, b11);
        asm volatile("s_waitcnt lgkmcnt(0)" ::: "memory");
        __builtin_amdgcn_sched_barrier(0);
        bf16x8 af[2], bfr[2];
        af[0] = bf16x8{a00[0], a00[1], a00[2], a00[3], a01[0], a01[1], a01[2], a01[3]};
        af[1] = bf16x8{a10[0], a10[1], a10[2], a10[3], a11[0], a11[1], a11[2], a11[3]};
        bfr[0] = bf16x8{b00[0], b00[1], b00[2], b00[3], b01[0], b01[1], b01[2], b01[3]};
        bfr[1] = bf16x8{b10[0], b10[1], b10[2], b10[3], b11[0], b11[1], b11[2], b11[3]};
#pragma unroll
        for (int i = 0; i < 2; ++i)
#pragma unroll
          for (int j = 0; j < 2; ++j)
            acc[i][j] = __builtin_amdgcn_mfma_f32_16x16x32_bf16(af[i], bfr[j], acc[i][j], 0, 0, 0);
      }
    }
#pragma unroll
    for (int i = 0; i < 2; ++i)
#pragma unroll
      for (int j = 0; j < 2; ++j) {
        const int f = n0 + wn + 16 * j + (lane & 15);
        const float bb = bf2f(b1[f]);
#pragma unroll
        for (int r = 0; r < 4; ++r) {
          const int d = m0 + wm + 16 * i + (lane >> 4) * 4 + r;
          float u = rbf(rbf(acc[i][j][r]) + bb);
          G[(size_t)d * 1792 + f] = f2bf(gelu_tanh(u));
        }
      }
    return;
  }
  const int d = blockIdx.x - 224;
  u16* cp = (u16*)smem;
  u16* Khi = (u16*)(smem + 35072);
  u16* Klo = (u16*)(smem + 35072 + 7680);
  float* red = (float*)(smem + 35072 + 15360);
  {
    bf16x8 uv = *(const bf16x8*)(xnT + (size_t)d * 2048 + tid * 8);
    bf16x8 kh = *(const bf16x8*)(Khg + (size_t)d * 2048 + tid * 8);
    bf16x8 kl = *(const bf16x8*)(Klg + (size_t)d * 2048 + tid * 8);
#pragma unroll
    for (int k = 0; k < 8; ++k)
#pragma unroll
      for (int e = 0; e < 8; ++e) cp[k * CP_STRIDE + 2048 + k - (tid * 8 + e)] = (u16)uv[e];
    if (tid < 8 * 32) {
      const int k = tid >> 5, xx = 2049 + k + (tid & 31);
      if (xx < 2080) cp[k * CP_STRIDE + xx] = 0;
    }
    const int c = (tid >> 2) + 32, w8 = (tid & 3) * 8;
    *(bf16x8*)(Khi + c * 40 + w8) = kh;
    *(bf16x8*)(Klo + c * 40 + w8) = kl;
    if (tid < 128) {
      const int c2 = tid >> 2;
      bf16x8 z = {};
      *(bf16x8*)(Khi + c2 * 40 + w8) = z;
      *(bf16x8*)(Klo + c2 * 40 + w8) = z;
    }
  }
  __syncthreads();
  f32x16 acc0 = {}, acc1 = {};
  {
    const int r = lane & 31, hh = lane >> 5;
    const u16* abase = cp + (r & 7) * CP_STRIDE + 2048 - 8 * (r >> 3) + 8 * hh;
    const int i = lane & 31;
    for (int m = wave; m < 64; m += 4) {
      bf16x8 a0 = *(const bf16x8*)(abase - 32 * m);
      bf16x8 a1 = *(const bf16x8*)(abase - 32 * m + 16);
      {
        const int ch = (32 + i - m + 32) * 40 + 8 * hh;
        bf16x8 bh0 = *(const bf16x8*)(Khi + ch), bh1 = *(const bf16x8*)(Khi + ch + 16);
        bf16x8 bl0 = *(const bf16x8*)(Klo + ch), bl1 = *(const bf16x8*)(Klo + ch + 16);
        acc1 = __builtin_amdgcn_mfma_f32_32x32x16_bf16(a0, bh0, acc1, 0, 0, 0);
        acc1 = __builtin_amdgcn_mfma_f32_32x32x16_bf16(a1, bh1, acc1, 0, 0, 0);
        acc1 = __builtin_amdgcn_mfma_f32_32x32x16_bf16(a0, bl0, acc1, 0, 0, 0);
        acc1 = __builtin_amdgcn_mfma_f32_32x32x16_bf16(a1, bl1, acc1, 0, 0, 0);
      }
      if (m < 32) {
        const int ch = (i - m + 32) * 40 + 8 * hh;
        bf16x8 bh0 = *(const bf16x8*)(Khi + ch), bh1 = *(const bf16x8*)(Khi + ch + 16);
        bf16x8 bl0 = *(const bf16x8*)(Klo + ch), bl1 = *(const bf16x8*)(Klo + ch + 16);
        acc0 = __builtin_amdgcn_mfma_f32_32x32x16_bf16(a0, bh0, acc0, 0, 0, 0);
        acc0 = __builtin_amdgcn_mfma_f32_32x32x16_bf16(a1, bh1, acc0, 0, 0, 0);
        acc0 = __builtin_amdgcn_mfma_f32_32x32x16_bf16(a0, bl0, acc0, 0, 0, 0);
        acc0 = __builtin_amdgcn_mfma_f32_32x32x16_bf16(a1, bl1, acc0, 0, 0, 0);
      }
    }
  }
  {
    const int i = lane & 31, hh = lane >> 5;
#pragma unroll
    for (int rg = 0; rg < 16; ++rg) {
      const int row = (rg & 3) + 8 * (rg >> 2) + 4 * hh;
      red[wave * 2112 + i * 33 + row] = acc0[rg];
      red[wave * 2112 + (32 + i) * 33 + row] = acc1[rg];
    }
  }
  __syncthreads();
  {
    bf16x8 o;
#pragma unroll
    for (int e = 0; e < 8; ++e) {
      const int l = tid * 8 + e;
      const int oo = (l >> 5) * 33 + (l & 31);
      float v = (red[oo] + red[2112 + oo]) + (red[2 * 2112 + oo] + red[3 * 2112 + oo]);
      o[e] = (short)f2bf(v);
    }
    *(bf16x8*)(yT + (size_t)d * 2048 + tid * 8) = o;
  }
}

__global__ __launch_bounds__(256) void gemm2_kernel(const u16* __restrict__ w2, const u16* __restrict__ G,
                                                    const u16* __restrict__ b2, const u16* __restrict__ x,
                                                    const u16* __restrict__ xn, const u16* __restrict__ yT,
                                                    const u16* __restrict__ dskip, u16* __restrict__ out) {
  __shared__ __attribute__((aligned(16))) char smem[NS * STAGE_BYTES];
  const int tid = threadIdx.x, wave = tid >> 6, lane = tid & 63;
  const int bn = blockIdx.x & 7, bm = blockIdx.x >> 3;
  const int m0 = bm * 64, n0 = bn * 64;
  const int wm = (wave >> 1) * 32, wn = (wave & 1) * 32;
  f32x4 acc[2][2] = {};
  const u16* srcA[2]; const u16* srcB[2];
#pragma unroll
  for (int i = 0; i < 2; ++i) {
    const unsigned row = wave * 16 + i * 8 + (lane >> 3);
    srcA[i] = w2 + (size_t)row * 2048 + m0 + kslow_src_chunk(row, lane & 7) * 8;
    srcB[i] = G + (size_t)(n0 + row) * 1792 + (((lane & 7) ^ (row & 7)) * 8);
  }
  auto issue = [&](int t) {
    char* st = smem + (t % NS) * STAGE_BYTES;
#pragma unroll
    for (int i = 0; i < 2; ++i) {
      GLDS(srcA[i] + (size_t)t * 64 * 2048, st + (wave * 16 + i * 8) * 128);
      GLDS(srcB[i] + t * 64, st + 8192 + (wave * 16 + i * 8) * 128);
    }
  };
  const int NT = 28;
#pragma unroll
  for (int s = 0; s < NS - 1; ++s) issue(s);
  const unsigned g = lane >> 4, q = (lane & 15) >> 2, p = lane & 3;
  for (int t = 0; t < NT; ++t) {
    if (t < NT - (NS - 1)) WAIT_VM(16); else WAIT_VM(0);
    __builtin_amdgcn_s_barrier();
    if (t + NS - 1 < NT) issue(t + NS - 1);
    char* st = smem + (t % NS) * STAGE_BYTES;
    const unsigned baseA = (unsigned)(size_t)st;
#pragma unroll
    for (int kk = 0; kk < 2; ++kk) {
      const unsigned r0 = kk * 32 + 8 * g + q;
      bf16x4 a00, a01, a10, a11;
      tr_read8(baseA + swz_off(r0, wm + 4 * p), baseA + swz_off(r0 + 4, wm + 4 * p),
               baseA + swz_off(r0, wm + 16 + 4 * p), baseA + swz_off(r0 + 4, wm + 16 + 4 * p), a00, a01, a10, a11);
      bf16x8 bfr[2];
#pragma unroll
      for (int j = 0; j < 2; ++j) {
        const unsigned rown = wn + 16 * j + (lane & 15);
        bfr[j] = *(const bf16x8*)(st + 8192 + rown * 128 + (((kk * 4 + (lane >> 4)) ^ (rown & 7)) << 4));
      }
      asm volatile("s_waitcnt lgkmcnt(0)" ::: "memory");
      __builtin_amdgcn_sched_barrier(0);
      bf16x8 af[2];
      af[0] = bf16x8{a00[0], a00[1], a00[2], a00[3], a01[0], a01[1], a01[2], a01[3]};
      af[1] = bf16x8{a10[0], a10[1], a10[2], a10[3], a11[0], a11[1], a11[2], a11[3]};
#pragma unroll
      for (int i = 0; i < 2; ++i)
#pragma unroll
        for (int j = 0; j < 2; ++j)
          acc[i][j] = __builtin_amdgcn_mfma_f32_16x16x32_bf16(af[i], bfr[j], acc[i][j], 0, 0, 0);
    }
  }
#pragma unroll
  for (int i = 0; i < 2; ++i)
#pragma unroll
    for (int j = 0; j < 2; ++j) {
      const int d = n0 + wn + 16 * j + (lane & 15);
      const float sk = bf2f(dskip[d]);
#pragma unroll
      for (int r = 0; r < 4; ++r) {
        const int s = m0 + wm + 16 * i + (lane >> 4) * 4 + r;
        float val = rbf(rbf(acc[i][j][r]) + bf2f(b2[s]));
        float a = rbf(bf2f(yT[(size_t)d * 2048 + s]) + rbf(bf2f(xn[(size_t)s * 512 + d]) * sk));
        float o = rbf(rbf(bf2f(x[(size_t)s * 512 + d]) + a) + val);
        out[(size_t)s * 512 + d] = f2bf(o);
      }
    }
}

extern "C" void kernel_launch(void* const* d_in, const int* in_sizes, int n_in,
                              void* d_out, int out_size, void* d_ws, size_t ws_size,
                              hipStream_t stream) {
  const u16* x = (const u16*)d_in[0];
  const u16* lar = (const u16*)d_in[1];
  const u16* aim = (const u16*)d_in[2];
  const u16* cr = (const u16*)d_in[3];
  const u16* ci = (const u16*)d_in[4];
  const u16* logdt = (const u16*)d_in[5];
  const u16* dskip = (const u16*)d_in[6];
  const u16* w1 = (const u16*)d_in[8];
  const u16* b1 = (const u16*)d_in[9];
  const u16* w2 = (const u16*)d_in[10];
  const u16* b2 = (const u16*)d_in[11];
  u16* out = (u16*)d_out;
  char* ws = (char*)d_ws;
  const size_t SZ = (size_t)2048 * 512 * 2;
  u16* xn = (u16*)(ws);
  u16* h = (u16*)(ws + SZ);
  u16* xnT = (u16*)(ws + 2 * SZ);
  u16* yT = (u16*)(ws + 3 * SZ);
  u16* Khi = (u16*)(ws + 4 * SZ);
  u16* Klo = (u16*)(ws + 5 * SZ);
  u16* G = (u16*)(ws + 6 * SZ);
  prep_kernel<<<128 + 512, 256, 0, stream>>>(x, xn, h, xnT, lar, aim, cr, ci, logdt, Khi, Klo);
  mid_kernel<<<224 + 512, 256, 0, stream>>>(h, w1, b1, G, xnT, Khi, Klo, yT);
  gemm2_kernel<<<256, 256, 0, stream>>>(w2, G, b2, x, xn, yT, dskip, out);
}
```

```cpp
#include <hip/hip_runtime.h>
#include <hip/hip_bf16.h>
#include <stdint.h>

typedef __attribute__((ext_vector_type(8))) short bf16x8;
typedef __attribute__((ext_vector_type(4))) short bf16x4;
typedef __attribute__((ext_vector_type(4))) float f32x4;
typedef __attribute__((ext_vector_type(16))) float f32x16;
typedef unsigned short u16;

__device__ __forceinline__ float bf2f(u16 v) { return __uint_as_float(((unsigned)v) << 16); }
__device__ __forceinline__ u16 f2bf(float f) {
  unsigned u = __float_as_uint(f);
  u += 0x7fffu + ((u >> 16) & 1u);
  return (u16)(u >> 16);
}
__device__ __forceinline__ float rbf(float f) { return bf2f(f2bf(f)); }

__device__ __forceinline__ float wave_sum(float v) {
#pragma unroll
  for (int o = 32; o > 0; o >>= 1) v += __shfl_xor(v, o, 64);
  return v;
}

__device__ __forceinline__ void ln8(const float* x, float* y) {
  float s = 0.f;
#pragma unroll
  for (int i = 0; i < 8; ++i) s += x[i];
  s = wave_sum(s);
  float mean = s * (1.0f / 512.0f);
  float q = 0.f;
#pragma unroll
  for (int i = 0; i < 8; ++i) { float d = x[i] - mean; q += d * d; }
  q = wave_sum(q);
  float var = q * (1.0f / 512.0f);
  float m_b = rbf(mean);
  float v_b = rbf(var);
  float den = rbf(sqrtf(rbf(v_b + 1e-5f)));
#pragma unroll
  for (int i = 0; i < 8; ++i) y[i] = rbf(rbf(x[i] - m_b) / den);
}

__device__ __forceinline__ int idx_of_l(int l) {
  int p = (int)rbf((float)l);
  if (p < 256) return p;
  if (p < 512) return 256 + ((p - 256) >> 1);
  if (p < 1024) return 384 + ((p - 512) >> 2);
  return 512 + ((p - 1024) >> 3);
}
__device__ __forceinline__ float2 cexp_acc(float re, float im) {
  float e = expf(re), s, c;
  sincosf(im, &s, &c);
  return make_float2(e * c, e * s);
}
__device__ __forceinline__ float2 cmul(float2 a, float2 b) {
  return make_float2(a.x * b.x - a.y * b.y, a.x * b.y + a.y * b.x);
}

__global__ __launch_bounds__(256) void prep_kernel(const u16* __restrict__ x, u16* __restrict__ xn,
                                                   u16* __restrict__ h, u16* __restrict__ xnT,
                                                   const u16* __restrict__ lar, const u16* __restrict__ aim,
                                                   const u16* __restrict__ cr, const u16* __restrict__ ci,
                                                   const u16* __restrict__ logdt, u16* __restrict__ Khi,
                                                   u16* __restrict__ Klo) {
  __shared__ __attribute__((aligned(16))) char smem[57344 + 2816];
  const int tid = threadIdx.x, wave = tid >> 6, lane = tid & 63;
  if (blockIdx.x < 128) {
    u16(*tr)[512 + 8] = (u16(*)[512 + 8])smem;
    const int s0 = blockIdx.x * 16;
    bf16x8 v[4];
#pragma unroll
    for (int rr = 0; rr < 4; ++rr) v[rr] = *(const bf16x8*)(x + (size_t)(s0 + wave * 4 + rr) * 512 + lane * 8);
#pragma unroll
    for (int rr = 0; rr < 4; ++rr) {
      const int r = wave * 4 + rr;
      const int s = s0 + r;
      float xf[8], a[8], b[8];
#pragma unroll
      for (int i = 0; i < 8; ++i) xf[i] = bf2f((u16)v[rr][i]);
      ln8(xf, a);
      ln8(a, b);
      bf16x8 oa, ob;
#pragma unroll
      for (int i = 0; i < 8; ++i) { oa[i] = (short)f2bf(a[i]); ob[i] = (short)f2bf(b[i]); }
      *(bf16x8*)(xn + (size_t)s * 512 + lane * 8) = oa;
      *(bf16x8*)(h + (size_t)s * 512 + lane * 8) = ob;
#pragma unroll
      for (int i = 0; i < 8; ++i) tr[r][lane * 8 + i] = (u16)oa[i];
    }
    __syncthreads();
    for (int idx = tid; idx < 512 * 2; idx += 256) {
      const int d = idx >> 1, c = idx & 1;
      bf16x8 o;
#pragma unroll
      for (int i = 0; i < 8; ++i) o[i] = (short)tr[c * 8 + i][d];
      *(bf16x8*)(xnT + (size_t)d * 2048 + s0 + c * 8) = o;
    }
    return;
  }
  const int d = blockIdx.x - 128;
  float2(*tabA)[24] = (float2(*)[24])smem;
  float2(*tabB)[32] = (float2(*)[32])(smem + 24576);
  float* Kd = (float*)(smem + 57344);
  const float dt = rbf(expf(bf2f(logdt[d])));
  const int n = tid & 127, half = tid >> 7;
  const float ar = -rbf(expf(bf2f(lar[d * 128 + n]))) * dt;
  const float ai = bf2f(aim[d * 128 + n]) * dt;
  const float2 Cc = make_float2(bf2f(cr[d * 128 + n]) * dt, bf2f(ci[d * 128 + n]) * dt);
  for (int pass = 0; pass < 2; ++pass) {
    const int R = pass * 2 + half;
    {
      const float bstep = (float)(1 << R);
      const float astep = (float)(16 << R);
      const float base = (R == 0) ? 0.f : (float)(128 << R);
      const int numA = (R == 0) ? 16 : (R == 3 ? 8 : 8);
      float2 e = cexp_acc(ar * bstep, ai * bstep);
      float2 p = make_float2(1.f, 0.f);
      for (int b = 0; b < 16; ++b) { tabB[n][half * 16 + b] = p; p = cmul(p, e); }
      e = cexp_acc(ar * astep, ai * astep);
      p = cmul(Cc, cexp_acc(ar * base, ai * base));
      for (int a = 0; a < numA; ++a) { tabA[n][half * 16 + a] = p; p = cmul(p, e); }
    }
    __syncthreads();
    if (pass == 0) {
      {
        const int a = tid >> 4, b = tid & 15;
        float acc = 0.f;
#pragma unroll 8
        for (int nn = 0; nn < 128; ++nn) { float2 xx = tabA[nn][a], yy = tabB[nn][b]; acc += xx.x * yy.x - xx.y * yy.y; }
        Kd[tid] = 2.0f * acc;
      }
      if (tid < 128) {
        const int a = tid >> 4, b = tid & 15;
        float acc = 0.f;
#pragma unroll 8
        for (int nn = 0; nn < 128; ++nn) { float2 xx = tabA[nn][16 + a], yy = tabB[nn][16 + b]; acc += xx.x * yy.x - xx.y * yy.y; }
        Kd[256 + tid] = 2.0f * acc;
      }
    } else {
      const int t = tid & 127;
      const int a = t >> 4, b = t & 15;
      float acc = 0.f;
#pragma unroll 8
      for (int nn = 0; nn < 128; ++nn) { float2 xx = tabA[nn][half * 16 + a], yy = tabB[nn][half * 16 + b]; acc += xx.x * yy.x - xx.y * yy.y; }
      Kd[384 + tid] = 2.0f * acc;
    }
    __syncthreads();
  }
  {
    float v = 0.f;
    if (tid < 128) { float2 p = cmul(Cc, cexp_acc(ar * 2048.f, ai * 2048.f)); v = p.x; }
    float* part = (float*)smem;
    if (tid < 128) part[tid] = v;
    __syncthreads();
    if (wave == 0) {
      float s = part[lane] + part[lane + 64];
      s = wave_sum(s);
      if (lane == 0) Kd[640] = 2.0f * s;
    }
    __syncthreads();
  }
  {
    bf16x8 hi, lo;
#pragma unroll
    for (int e = 0; e < 8; ++e) {
      float kv = Kd[idx_of_l(tid * 8 + e)];
      u16 hv = f2bf(kv);
      hi[e] = (short)hv;
      lo[e] = (short)f2bf(kv - bf2f(hv));
    }
    *(bf16x8*)(Khi + (size_t)d * 2048 + tid * 8) = hi;
    *(bf16x8*)(Klo + (size_t)d * 2048 + tid * 8) = lo;
  }
}

__device__ __forceinline__ unsigned kslow_src_chunk(unsigned row, unsigned c16) {
  unsigned f = ((row >> 1) & 1) | (((row >> 3) & 1) << 1);
  return ((((c16 >> 1) ^ f)) << 1) | (c16 & 1);
}
__device__ __forceinline__ unsigned swz_off(unsigned row, unsigned col) {
  unsigned c32 = col >> 4;
  unsigned f = ((row >> 1) & 1) | (((row >> 3) & 1) << 1);
  return row * 128 + ((c32 ^ f) << 5) + ((col & 15) << 1);
}
__device__ __forceinline__ float gelu_tanh(float x) {
  float k = 0.7978845608028654f * (x + 0.044715f * x * x * x);
  return 0.5f * x * (1.0f + tanhf(k));
}
#define GLDS(src, dst) __builtin_amdgcn_global_load_lds((const unsigned*)(src), (unsigned*)(dst), 16, 0, 0)
#define NS 4
#define STAGE_BYTES 32768
#define WAIT_VM(n) asm volatile("s_waitcnt vmcnt(" #n ")" ::: "memory")

__device__ __forceinline__ void tr_read8(unsigned a0, unsigned a1, unsigned a2, unsigned a3,
                                         bf16x4& r0, bf16x4& r1, bf16x4& r2, bf16x4& r3) {
  asm volatile("ds_read_b64_tr_b16 %0, %4\n\tds_read_b64_tr_b16 %1, %5\n\tds_read_b64_tr_b16 %2, %6\n\tds_read_b64_tr_b16 %3, %7"
               : "=&v"(r0), "=&v"(r1), "=&v"(r2), "=&v"(r3) : "v"(a0), "v"(a1), "v"(a2), "v"(a3) : "memory");
}

__device__ __forceinline__ void b128_read2(unsigned a0, unsigned a1, bf16x8& r0, bf16x8& r1) {
  asm volatile("ds_read_b128 %0, %2\n\tds_read_b128 %1, %3" : "=&v"(r0), "=&v"(r1) : "v"(a0), "v"(a1) : "memory");
}
struct FragA { bf16x4 v[2][4]; };
__device__ __forceinline__ bf16x8 cat4(bf16x4 a, bf16x4 b) { return bf16x8{a[0], a[1], a[2], a[3], b[0], b[1], b[2], b[3]}; }

#define CP_STRIDE 2192

__global__ __launch_bounds__(256) void mid_kernel(const u16* __restrict__ h, const u16* __restrict__ w1,
                                                  const u16* __restrict__ b1, u16* __restrict__ G,
                                                  const u16* __restrict__ xnT, const u16* __restrict__ Khg,
                                                  const u16* __restrict__ Klg, u16* __restrict__ yT) {
  __shared__ __attribute__((aligned(16))) char smem[NS * STAGE_BYTES];
  const int tid = threadIdx.x, wave = tid >> 6, lane = tid & 63;
  if (blockIdx.x < 224) {
    const int tile = (blockIdx.x & 7) * 28 + (blockIdx.x >> 3);
    const int bm = tile & 7, bn = tile >> 3;
    const int m0 = bm * 64, n0 = bn * 64;
    f32x4 acc[4][4] = {};
    const u16* srcA[4]; const u16* srcB[4];
#pragma unroll
    for (int i = 0; i < 4; ++i) {
      const unsigned row = wave * 32 + i * 8 + (lane >> 3);
      const unsigned sc = kslow_src_chunk(row, lane & 7);
      srcA[i] = h + (size_t)row * 512 + m0 + sc * 8;
      srcB[i] = w1 + (size_t)row * 1792 + n0 + sc * 8;
    }
    auto issue = [&](int t) {
      char* st = smem + (t % NS) * STAGE_BYTES;
#pragma unroll
      for (int i = 0; i < 4; ++i) {
        GLDS(srcA[i] + (size_t)t * 128 * 512, st + (wave * 32 + i * 8) * 128);
        GLDS(srcB[i] + (size_t)t * 128 * 1792, st + 16384 + (wave * 32 + i * 8) * 128);
      }
    };
    const int NT = 16;
#pragma unroll
    for (int s = 0; s < NS - 1; ++s) issue(s);
    const unsigned g = lane >> 4, q = (lane & 15) >> 2, p = lane & 3;
    const unsigned r0 = wave * 32 + 8 * g + q;
    struct F8 { bf16x4 v[8]; };
    F8 fa[2], fb[2];
    auto load_half = [&](F8& A, unsigned base) {
      tr_read8(base + swz_off(r0, 4 * p), base + swz_off(r0 + 4, 4 * p),
               base + swz_off(r0, 16 + 4 * p), base + swz_off(r0 + 4, 16 + 4 * p), A.v[0], A.v[1], A.v[2], A.v[3]);
      tr_read8(base + swz_off(r0, 32 + 4 * p), base + swz_off(r0 + 4, 32 + 4 * p),
               base + swz_off(r0, 48 + 4 * p), base + swz_off(r0 + 4, 48 + 4 * p), A.v[4], A.v[5], A.v[6], A.v[7]);
    };
    auto compute = [&](F8& A, F8& B) {
#pragma unroll
      for (int i = 0; i < 4; ++i) {
        bf16x8 a = cat4(A.v[2 * i], A.v[2 * i + 1]);
#pragma unroll
        for (int j = 0; j < 4; ++j)
          acc[i][j] = __builtin_amdgcn_mfma_f32_16x16x32_bf16(a, cat4(B.v[2 * j], B.v[2 * j + 1]), acc[i][j], 0, 0, 0);
      }
    };
    auto step = [&](int t, F8& Ac, F8& Bc, F8& An, F8& Bn) {
      if (t + NS - 1 <= NT) WAIT_VM(8); else WAIT_VM(0);
      __builtin_amdgcn_s_barrier();
      if (t + NS - 1 < NT) issue(t + NS - 1);
      if (t + 1 < NT) {
        const unsigned base = (unsigned)(size_t)(smem + ((t + 1) % NS) * STAGE_BYTES);
        load_half(An, base);
        asm volatile("s_waitcnt lgkmcnt(8)" ::: "memory");
        load_half(Bn, base + 16384);
      } else {
        asm volatile("s_waitcnt lgkmcnt(0)" ::: "memory");
      }
      __builtin_amdgcn_sched_barrier(0);
      compute(Ac, Bc);
      __builtin_amdgcn_sched_barrier(0);
    };
    WAIT_VM(16);
    __builtin_amdgcn_s_barrier();
    load_half(fa[0], (unsigned)(size_t)smem);
    load_half(fb[0], (unsigned)(size_t)smem + 16384);
    for (int t = 0; t < NT; t += 2) {
      step(t, fa[0], fb[0], fa[1], fb[1]);
      step(t + 1, fa[1], fb[1], fa[0], fb[0]);
    }
    __syncthreads();
    float* red = (float*)smem;
#pragma unroll
    for (int i = 0; i < 4; ++i)
#pragma unroll
      for (int j = 0; j < 4; ++j)
#pragma unroll
        for (int r = 0; r < 4; ++r)
          red[wave * 4352 + (16 * i + (lane >> 4) * 4 + r) * 68 + 16 * j + (lane & 15)] = acc[i][j][r];
    __syncthreads();
    {
      const int row = tid >> 2, c0 = (tid & 3) * 16;
      bf16x8 o[2];
#pragma unroll
      for (int c = 0; c < 16; ++c) {
        const int o_ = row * 68 + c0 + c;
        float v = (red[o_] + red[4352 + o_]) + (red[2 * 4352 + o_] + red[3 * 4352 + o_]);
        float u = rbf(rbf(v) + bf2f(b1[n0 + c0 + c]));
        o[c >> 3][c & 7] = (short)f2bf(gelu_tanh(u));
      }
      bf16x8* dst = (bf16x8*)(G + (size_t)(m0 + row) * 1792 + n0 + c0);
      dst[0] = o[0]; dst[1] = o[1];
    }
    return;
  }
  const int d = blockIdx.x - 224;
  u16* cp = (u16*)smem;
  u16* Khi = (u16*)(smem + 35072);
  u16* Klo = (u16*)(smem + 35072 + 7680);
  float* red = (float*)(smem + 35072 + 15360);
  {
    bf16x8 uv = *(const bf16x8*)(xnT + (size_t)d * 2048 + tid * 8);
    bf16x8 kh = *(const bf16x8*)(Khg + (size_t)d * 2048 + tid * 8);
    bf16x8 kl = *(const bf16x8*)(Klg + (size_t)d * 2048 + tid * 8);
#pragma unroll
    for (int k = 0; k < 8; ++k)
#pragma unroll
      for (int e = 0; e < 8; ++e) cp[k * CP_STRIDE + 2048 + k - (tid * 8 + e)] = (u16)uv[e];
    if (tid < 8 * 32) {
      const int k = tid >> 5, xx = 2049 + k + (tid & 31);
      if (xx < 2080) cp[k * CP_STRIDE + xx] = 0;
    }
    const int c = (tid >> 2) + 32, w8 = (tid & 3) * 8;
    *(bf16x8*)(Khi + c * 40 + w8) = kh;
    *(bf16x8*)(Klo + c * 40 + w8) = kl;
    if (tid < 128) {
      const int c2 = tid >> 2;
      bf16x8 z = {};
      *(bf16x8*)(Khi + c2 * 40 + w8) = z;
      *(bf16x8*)(Klo + c2 * 40 + w8) = z;
    }
  }
  __syncthreads();
  f32x16 acc0 = {}, acc1 = {};
  {
    const int r = lane & 31, hh = lane >> 5;
    const u16* abase = cp + (r & 7) * CP_STRIDE + 2048 - 8 * (r >> 3) + 8 * hh;
    const int i = lane & 31;
    for (int m = wave; m < 64; m += 4) {
      bf16x8 a0 = *(const bf16x8*)(abase - 32 * m);
      bf16x8 a1 = *(const bf16x8*)(abase - 32 * m + 16);
      {
        const int ch = (32 + i - m + 32) * 40 + 8 * hh;
        bf16x8 bh0 = *(const bf16x8*)(Khi + ch), bh1 = *(const bf16x8*)(Khi + ch + 16);
        bf16x8 bl0 = *(const bf16x8*)(Klo + ch), bl1 = *(const bf16x8*)(Klo + ch + 16);
        acc1 = __builtin_amdgcn_mfma_f32_32x32x16_bf16(a0, bh0, acc1, 0, 0, 0);
        acc1 = __builtin_amdgcn_mfma_f32_32x32x16_bf16(a1, bh1, acc1, 0, 0, 0);
        acc1 = __builtin_amdgcn_mfma_f32_32x32x16_bf16(a0, bl0, acc1, 0, 0, 0);
        acc1 = __builtin_amdgcn_mfma_f32_32x32x16_bf16(a1, bl1, acc1, 0, 0, 0);
      }
      if (m < 32) {
        const int ch = (i - m + 32) * 40 + 8 * hh;
        bf16x8 bh0 = *(const bf16x8*)(Khi + ch), bh1 = *(const bf16x8*)(Khi + ch + 16);
        bf16x8 bl0 = *(const bf16x8*)(Klo + ch), bl1 = *(const bf16x8*)(Klo + ch + 16);
        acc0 = __builtin_amdgcn_mfma_f32_32x32x16_bf16(a0, bh0, acc0, 0, 0, 0);
        acc0 = __builtin_amdgcn_mfma_f32_32x32x16_bf16(a1, bh1, acc0, 0, 0, 0);
        acc0 = __builtin_amdgcn_mfma_f32_32x32x16_bf16(a0, bl0, acc0, 0, 0, 0);
        acc0 = __builtin_amdgcn_mfma_f32_32x32x16_bf16(a1, bl1, acc0, 0, 0, 0);
      }
    }
  }
  {
    const int i = lane & 31, hh = lane >> 5;
#pragma unroll
    for (int rg = 0; rg < 16; ++rg) {
      const int row = (rg & 3) + 8 * (rg >> 2) + 4 * hh;
      red[wave * 2112 + i * 33 + row] = acc0[rg];
      red[wave * 2112 + (32 + i) * 33 + row] = acc1[rg];
    }
  }
  __syncthreads();
  {
    bf16x8 o;
#pragma unroll
    for (int e = 0; e < 8; ++e) {
      const int l = tid * 8 + e;
      const int oo = (l >> 5) * 33 + (l & 31);
      float v = (red[oo] + red[2112 + oo]) + (red[2 * 2112 + oo] + red[3 * 2112 + oo]);
      o[e] = (short)f2bf(v);
    }
    *(bf16x8*)(yT + (size_t)d * 2048 + tid * 8) = o;
  }
}

__global__ __launch_bounds__(256) void gemm2_kernel(const u16* __restrict__ w2, const u16* __restrict__ G,
                                                    const u16* __restrict__ b2, const u16* __restrict__ x,
                                                    const u16* __restrict__ xn, const u16* __restrict__ yT,
                                                    const u16* __restrict__ dskip, u16* __restrict__ out) {
  __shared__ __attribute__((aligned(16))) char smem[NS * STAGE_BYTES];
  const int tid = threadIdx.x, wave = tid >> 6, lane = tid & 63;
  const int bn = (blockIdx.x >> 3) & 7, bm = (blockIdx.x & 7) * 4 + (blockIdx.x >> 6);
  const int m0 = bm * 64, n0 = bn * 64;
  f32x4 acc[4][4] = {};
  const u16* srcA[4]; const u16* srcB[4];
#pragma unroll
  for (int i = 0; i < 4; ++i) {
    const unsigned row = wave * 32 + i * 8 + (lane >> 3);
    srcA[i] = w2 + (size_t)row * 2048 + m0 + kslow_src_chunk(row, lane & 7) * 8;
    const unsigned nr = row & 63, kh = row >> 6;
    srcB[i] = G + (size_t)(n0 + nr) * 1792 + kh * 64 + (((lane & 7) ^ (nr & 7)) * 8);
  }
  auto issue = [&](int t) {
    char* st = smem + (t % NS) * STAGE_BYTES;
#pragma unroll
    for (int i = 0; i < 4; ++i) {
      GLDS(srcA[i] + (size_t)t * 128 * 2048, st + (wave * 32 + i * 8) * 128);
      GLDS(srcB[i] + t * 128, st + 16384 + (wave * 32 + i * 8) * 128);
    }
  };
  const int NT = 14;
#pragma unroll
  for (int s = 0; s < NS - 1; ++s) issue(s);
  const unsigned g = lane >> 4, q = (lane & 15) >> 2, p = lane & 3;
  const unsigned r0 = wave * 32 + 8 * g + q;
  struct F8 { bf16x4 v[8]; };
  struct FB { bf16x8 v[4]; };
  F8 fa[2]; FB fb[2];
  auto loadA = [&](F8& A, unsigned base) {
    tr_read8(base + swz_off(r0, 4 * p), base + swz_off(r0 + 4, 4 * p),
             base + swz_off(r0, 16 + 4 * p), base + swz_off(r0 + 4, 16 + 4 * p), A.v[0], A.v[1], A.v[2], A.v[3]);
    tr_read8(base + swz_off(r0, 32 + 4 * p), base + swz_off(r0 + 4, 32 + 4 * p),
             base + swz_off(r0, 48 + 4 * p), base + swz_off(r0 + 4, 48 + 4 * p), A.v[4], A.v[5], A.v[6], A.v[7]);
  };
  auto loadB = [&](FB& B, unsigned base) {
    const unsigned bb = base + (wave >> 1) * 8192;
    const unsigned ch = (wave & 1) * 4 + (lane >> 4);
    const unsigned n_0 = (lane & 15), n_1 = n_0 + 16, n_2 = n_0 + 32, n_3 = n_0 + 48;
    b128_read2(bb + n_0 * 128 + ((ch ^ (n_0 & 7)) << 4), bb + n_1 * 128 + ((ch ^ (n_1 & 7)) << 4), B.v[0], B.v[1]);
    b128_read2(bb + n_2 * 128 + ((ch ^ (n_2 & 7)) << 4), bb + n_3 * 128 + ((ch ^ (n_3 & 7)) << 4), B.v[2], B.v[3]);
  };
  auto compute = [&](F8& A, FB& B) {
#pragma unroll
    for (int i = 0; i < 4; ++i) {
      bf16x8 a = cat4(A.v[2 * i], A.v[2 * i + 1]);
#pragma unroll
      for (int j = 0; j < 4; ++j)
        acc[i][j] = __builtin_amdgcn_mfma_f32_16x16x32_bf16(a, B.v[j], acc[i][j], 0, 0, 0);
    }
  };
  auto step = [&](int t, F8& Ac, FB& Bc, F8& An, FB& Bn) {
    if (t + NS - 1 <= NT) WAIT_VM(8); else WAIT_VM(0);
    __builtin_amdgcn_s_barrier();
    if (t + NS - 1 < NT) issue(t + NS - 1);
    if (t + 1 < NT) {
      const unsigned base = (unsigned)(size_t)(smem + ((t + 1) % NS) * STAGE_BYTES);
      loadA(An, base);
      loadB(Bn, base + 16384);
      asm volatile("s_waitcnt lgkmcnt(12)" ::: "memory");
    } else {
      asm volatile("s_waitcnt lgkmcnt(0)" ::: "memory");
    }
    __builtin_amdgcn_sched_barrier(0);
    compute(Ac, Bc);
    __builtin_amdgcn_sched_barrier(0);
  };
  WAIT_VM(16);
  __builtin_amdgcn_s_barrier();
  loadA(fa[0], (unsigned)(size_t)smem);
  loadB(fb[0], (unsigned)(size_t)smem + 16384);
  for (int t = 0; t < NT; t += 2) {
    step(t, fa[0], fb[0], fa[1], fb[1]);
    step(t + 1, fa[1], fb[1], fa[0], fb[0]);
  }
  __syncthreads();
  float* red = (float*)smem;
#pragma unroll
  for (int i = 0; i < 4; ++i)
#pragma unroll
    for (int j = 0; j < 4; ++j)
#pragma unroll
      for (int r = 0; r < 4; ++r)
        red[wave * 4352 + (16 * i + (lane >> 4) * 4 + r) * 68 + 16 * j + (lane & 15)] = acc[i][j][r];
  __syncthreads();
  {
    const int row = tid >> 2, c0 = (tid & 3) * 16;
    const int s = m0 + row;
    const float bb2 = bf2f(b2[s]);
    const bf16x8* xp = (const bf16x8*)(x + (size_t)s * 512 + n0 + c0);
    const bf16x8* xnp = (const bf16x8*)(xn + (size_t)s * 512 + n0 + c0);
    const bf16x8* skp = (const bf16x8*)(dskip + n0 + c0);
    bf16x8 xv[2] = {xp[0], xp[1]}, xnv[2] = {xnp[0], xnp[1]}, skv[2] = {skp[0], skp[1]};
    bf16x8 o[2];
#pragma unroll
    for (int c = 0; c < 16; ++c) {
      const int o_ = row * 68 + c0 + c;
      float v = (red[o_] + red[4352 + o_]) + (red[2 * 4352 + o_] + red[3 * 4352 + o_]);
      float val = rbf(rbf(v) + bb2);
      float a = rbf(bf2f(yT[(size_t)(n0 + c0 + c) * 2048 + s]) + rbf(bf2f((u16)xnv[c >> 3][c & 7]) * bf2f((u16)skv[c >> 3][c & 7])));
      float ov = rbf(rbf(bf2f((u16)xv[c >> 3][c & 7]) + a) + val);
      o[c >> 3][c & 7] = (short)f2bf(ov);
    }
    bf16x8* dst = (bf16x8*)(out + (size_t)s * 512 + n0 + c0);
    dst[0] = o[0]; dst[1] = o[1];
  }
}

extern "C" void kernel_launch(void* const* d_in, const int* in_sizes, int n_in,
                              void* d_out, int out_size, void* d_ws, size_t ws_size,
                              hipStream_t stream) {
  const u16* x = (const u16*)d_in[0];
  const u16* lar = (const u16*)d_in[1];
  const u16* aim = (const u16*)d_in[2];
  const u16* cr = (const u16*)d_in[3];
  const u16* ci = (const u16*)d_in[4];
  const u16* logdt = (const u16*)d_in[5];
  const u16* dskip = (const u16*)d_in[6];
  const u16* w1 = (const u16*)d_in[8];
  const u16* b1 = (const u16*)d_in[9];
  const u16* w2 = (const u16*)d_in[10];
  const u16* b2 = (const u16*)d_in[11];
  u16* out = (u16*)d_out;
  char* ws = (char*)d_ws;
  const size_t SZ = (size_t)2048 * 512 * 2;
  u16* xn = (u16*)(ws);
  u16* h = (u16*)(ws + SZ);
  u16* xnT = (u16*)(ws + 2 * SZ);
  u16* yT = (u16*)(ws + 3 * SZ);
  u16* Khi = (u16*)(ws + 4 * SZ);
  u16* Klo = (u16*)(ws + 5 * SZ);
  u16* G = (u16*)(ws + 6 * SZ);
  prep_kernel<<<128 + 512, 256, 0, stream>>>(x, xn, h, xnT, lar, aim, cr, ci, logdt, Khi, Klo);
  mid_kernel<<<224 + 512, 256, 0, stream>>>(h, w1, b1, G, xnT, Khi, Klo, yT);
  gemm2_kernel<<<256, 256, 0, stream>>>(w2, G, b2, x, xn, yT, dskip, out);
}
```

```cpp
#include <hip/hip_runtime.h>
#include <hip/hip_bf16.h>
#include <stdint.h>

typedef __attribute__((ext_vector_type(8))) short bf16x8;
typedef __attribute__((ext_vector_type(4))) short bf16x4;
typedef __attribute__((ext_vector_type(4))) float f32x4;
typedef __attribute__((ext_vector_type(16))) float f32x16;
typedef unsigned short u16;

__device__ __forceinline__ float bf2f(u16 v) { return __uint_as_float(((unsigned)v) << 16); }
__device__ __forceinline__ u16 f2bf(float f) {
  unsigned u = __float_as_uint(f);
  u += 0x7fffu + ((u >> 16) & 1u);
  return (u16)(u >> 16);
}
__device__ __forceinline__ float rbf(float f) { return bf2f(f2bf(f)); }

__device__ __forceinline__ float wave_sum(float v) {
#pragma unroll
  for (int o = 32; o > 0; o >>= 1) v += __shfl_xor(v, o, 64);
  return v;
}

__device__ __forceinline__ void ln8(const float* x, float* y) {
  float s = 0.f;
#pragma unroll
  for (int i = 0; i < 8; ++i) s += x[i];
  s = wave_sum(s);
  float mean = s * (1.0f / 512.0f);
  float q = 0.f;
#pragma unroll
  for (int i = 0; i < 8; ++i) { float d = x[i] - mean; q += d * d; }
  q = wave_sum(q);
  float var = q * (1.0f / 512.0f);
  float m_b = rbf(mean);
  float v_b = rbf(var);
  float den = rbf(sqrtf(rbf(v_b + 1e-5f)));
#pragma unroll
  for (int i = 0; i < 8; ++i) y[i] = rbf(rbf(x[i] - m_b) / den);
}

__device__ __forceinline__ int idx_of_l(int l) {
  int p = (int)rbf((float)l);
  if (p < 256) return p;
  if (p < 512) return 256 + ((p - 256) >> 1);
  if (p < 1024) return 384 + ((p - 512) >> 2);
  return 512 + ((p - 1024) >> 3);
}
__device__ __forceinline__ float2 cexp_acc(float re, float im) {
  float e = expf(re), s, c;
  sincosf(im, &s, &c);
  return make_float2(e * c, e * s);
}
__device__ __forceinline__ float2 cmul(float2 a, float2 b) {
  return make_float2(a.x * b.x - a.y * b.y, a.x * b.y + a.y * b.x);
}

__global__ __launch_bounds__(256) void prep_kernel(const u16* __restrict__ x, u16* __restrict__ xn,
                                                   u16* __restrict__ h, u16* __restrict__ xnT,
                                                   const u16* __restrict__ lar, const u16* __restrict__ aim,
                                                   const u16* __restrict__ cr, const u16* __restrict__ ci,
                                                   const u16* __restrict__ logdt, float* __restrict__ Kdg) {
  __shared__ __attribute__((aligned(16))) char smem[2 * 16896 + 4096];
  const int tid = threadIdx.x, wave = tid >> 6, lane = tid & 63;
  if (blockIdx.x < 256) {
    u16(*tr)[512 + 8] = (u16(*)[512 + 8])smem;
    const int s0 = blockIdx.x * 8;
    bf16x8 v[2];
#pragma unroll
    for (int rr = 0; rr < 2; ++rr) v[rr] = *(const bf16x8*)(x + (size_t)(s0 + wave * 2 + rr) * 512 + lane * 8);
#pragma unroll
    for (int rr = 0; rr < 2; ++rr) {
      const int r = wave * 2 + rr;
      const int s = s0 + r;
      float xf[8], a[8], b[8];
#pragma unroll
      for (int i = 0; i < 8; ++i) xf[i] = bf2f((u16)v[rr][i]);
      ln8(xf, a);
      ln8(a, b);
      bf16x8 oa, ob;
#pragma unroll
      for (int i = 0; i < 8; ++i) { oa[i] = (short)f2bf(a[i]); ob[i] = (short)f2bf(b[i]); }
      *(bf16x8*)(xn + (size_t)s * 512 + lane * 8) = oa;
      *(bf16x8*)(h + (size_t)s * 512 + lane * 8) = ob;
      *(bf16x8*)(&tr[r][lane * 8]) = oa;
    }
    __syncthreads();
#pragma unroll
    for (int it = 0; it < 2; ++it) {
      const int d = tid + it * 256;
      bf16x8 o;
#pragma unroll
      for (int i = 0; i < 8; ++i) o[i] = (short)tr[i][d];
      *(bf16x8*)(xnT + (size_t)d * 2048 + s0) = o;
    }
    return;
  }
  const int idx = blockIdx.x - 256;
  const int d = idx >> 2, R = idx & 3;
  float(*tabA)[264] = (float(*)[264])smem;
  float(*tabB)[264] = (float(*)[264])(smem + 16896);
  float* red = (float*)(smem + 2 * 16896);
  const float dt = rbf(expf(bf2f(logdt[d])));
  const int n = tid & 127;
  const float ar = -rbf(expf(bf2f(lar[d * 128 + n]))) * dt;
  const float ai = bf2f(aim[d * 128 + n]) * dt;
  const int numA = (R == 0) ? 16 : (R == 3 ? 9 : 8);
  if (tid < 128) {
    const float bstep = (float)(1 << R);
    float2 e = cexp_acc(ar * bstep, ai * bstep);
    float2 p = make_float2(1.f, 0.f);
#pragma unroll
    for (int b = 0; b < 16; ++b) { *(float2*)&tabB[b][2 * n] = p; p = cmul(p, e); }
  } else {
    const float2 Cc = make_float2(bf2f(cr[d * 128 + n]) * dt, bf2f(ci[d * 128 + n]) * dt);
    const float astep = (float)(16 << R);
    const float base = (R == 0) ? 0.f : (float)(128 << R);
    float2 e = cexp_acc(ar * astep, ai * astep);
    float2 p = cmul(Cc, cexp_acc(ar * base, ai * base));
#pragma unroll
    for (int a = 0; a < 16; ++a) {
      *(float2*)&tabA[a][2 * n] = (a < numA) ? make_float2(p.x, -p.y) : make_float2(0.f, 0.f);
      p = (a < numA) ? cmul(p, e) : p;
    }
  }
  __syncthreads();
  {
    f32x4 acc = {};
    const int i = lane & 15, kq = lane >> 4;
#pragma unroll
    for (int st = 0; st < 16; ++st) {
      const int k = wave * 64 + st * 4 + kq;
      acc = __builtin_amdgcn_mfma_f32_16x16x4f32(tabA[i][k], tabB[i][k], acc, 0, 0, 0);
    }
#pragma unroll
    for (int r = 0; r < 4; ++r) red[wave * 256 + ((lane >> 4) * 4 + r) * 16 + (lane & 15)] = acc[r];
  }
  __syncthreads();
  const int nout = (R == 0) ? 256 : (R == 3 ? 129 : 128);
  if (tid < nout) {
    const int off = (R == 0) ? 0 : 128 + 128 * R;
    Kdg[d * 648 + off + tid] = 2.0f * ((red[tid] + red[256 + tid]) + (red[512 + tid] + red[768 + tid]));
  }
}

__device__ __forceinline__ unsigned kslow_src_chunk(unsigned row, unsigned c16) {
  unsigned f = ((row >> 1) & 1) | (((row >> 3) & 1) << 1);
  return ((((c16 >> 1) ^ f)) << 1) | (c16 & 1);
}
__device__ __forceinline__ unsigned swz_off(unsigned row, unsigned col) {
  unsigned c32 = col >> 4;
  unsigned f = ((row >> 1) & 1) | (((row >> 3) & 1) << 1);
  return row * 128 + ((c32 ^ f) << 5) + ((col & 15) << 1);
}
__device__ __forceinline__ float gelu_tanh(float x) {
  float k = 0.7978845608028654f * (x + 0.044715f * x * x * x);
  return 0.5f * x * (1.0f + tanhf(k));
}
#define GLDS(src, dst) __builtin_amdgcn_global_load_lds((const unsigned*)(src), (unsigned*)(dst), 16, 0, 0)
#define NS 4
#define STAGE_BYTES 32768
#define WAIT_VM(n) asm volatile("s_waitcnt vmcnt(" #n ")" ::: "memory")

__device__ __forceinline__ void tr_read8(unsigned a0, unsigned a1, unsigned a2, unsigned a3,
                                         bf16x4& r0, bf16x4& r1, bf16x4& r2, bf16x4& r3) {
  asm volatile("ds_read_b64_tr_b16 %0, %4\n\tds_read_b64_tr_b16 %1, %5\n\tds_read_b64_tr_b16 %2, %6\n\tds_read_b64_tr_b16 %3, %7"
               : "=&v"(r0), "=&v"(r1), "=&v"(r2), "=&v"(r3) : "v"(a0), "v"(a1), "v"(a2), "v"(a3) : "memory");
}

__device__ __forceinline__ void b128_read2(unsigned a0, unsigned a1, bf16x8& r0, bf16x8& r1) {
  asm volatile("ds_read_b128 %0, %2\n\tds_read_b128 %1, %3" : "=&v"(r0), "=&v"(r1) : "v"(a0), "v"(a1) : "memory");
}
struct FragA { bf16x4 v[2][4]; };
__device__ __forceinline__ bf16x8 cat4(bf16x4 a, bf16x4 b) { return bf16x8{a[0], a[1], a[2], a[3], b[0], b[1], b[2], b[3]}; }

#define CP_STRIDE 2192

__global__ __launch_bounds__(256) void mid_kernel(const u16* __restrict__ h, const u16* __restrict__ w1,
                                                  const u16* __restrict__ b1, u16* __restrict__ G,
                                                  const u16* __restrict__ xnT, const float* __restrict__ Kdg,
                                                  u16* __restrict__ yT) {
  __shared__ __attribute__((aligned(16))) char smem[NS * STAGE_BYTES];
  const int tid = threadIdx.x, wave = tid >> 6, lane = tid & 63;
  if (blockIdx.x < 224) {
    const int tile = (blockIdx.x & 7) * 28 + (blockIdx.x >> 3);
    const int bm = tile & 7, bn = tile >> 3;
    const int m0 = bm * 64, n0 = bn * 64;
    f32x4 acc[4][4] = {};
    const u16* srcA[4]; const u16* srcB[4];
#pragma unroll
    for (int i = 0; i < 4; ++i) {
      const unsigned row = wave * 32 + i * 8 + (lane >> 3);
      const unsigned sc = kslow_src_chunk(row, lane & 7);
      srcA[i] = h + (size_t)row * 512 + m0 + sc * 8;
      srcB[i] = w1 + (size_t)row * 1792 + n0 + sc * 8;
    }
    auto issue = [&](int t) {
      char* st = smem + (t % NS) * STAGE_BYTES;
#pragma unroll
      for (int i = 0; i < 4; ++i) {
        GLDS(srcA[i] + (size_t)t * 128 * 512, st + (wave * 32 + i * 8) * 128);
        GLDS(srcB[i] + (size_t)t * 128 * 1792, st + 16384 + (wave * 32 + i * 8) * 128);
      }
    };
    const int NT = 16;
#pragma unroll
    for (int s = 0; s < NS - 1; ++s) issue(s);
    const unsigned g = lane >> 4, q = (lane & 15) >> 2, p = lane & 3;
    const unsigned r0 = wave * 32 + 8 * g + q;
    struct F8 { bf16x4 v[8]; };
    F8 fa[2], fb[2];
    auto load_half = [&](F8& A, unsigned base) {
      tr_read8(base + swz_off(r0, 4 * p), base + swz_off(r0 + 4, 4 * p),
               base + swz_off(r0, 16 + 4 * p), base + swz_off(r0 + 4, 16 + 4 * p), A.v[0], A.v[1], A.v[2], A.v[3]);
      tr_read8(base + swz_off(r0, 32 + 4 * p), base + swz_off(r0 + 4, 32 + 4 * p),
               base + swz_off(r0, 48 + 4 * p), base + swz_off(r0 + 4, 48 + 4 * p), A.v[4], A.v[5], A.v[6], A.v[7]);
    };
    auto compute = [&](F8& A, F8& B) {
#pragma unroll
      for (int i = 0; i < 4; ++i) {
        bf16x8 a = cat4(A.v[2 * i], A.v[2 * i + 1]);
#pragma unroll
        for (int j = 0; j < 4; ++j)
          acc[i][j] = __builtin_amdgcn_mfma_f32_16x16x32_bf16(a, cat4(B.v[2 * j], B.v[2 * j + 1]), acc[i][j], 0, 0, 0);
      }
    };
    auto step = [&](int t, F8& Ac, F8& Bc, F8& An, F8& Bn) {
      if (t + NS - 1 <= NT) WAIT_VM(8); else WAIT_VM(0);
      __builtin_amdgcn_s_barrier();
      if (t + NS - 1 < NT) issue(t + NS - 1);
      if (t + 1 < NT) {
        const unsigned base = (unsigned)(size_t)(smem + ((t + 1) % NS) * STAGE_BYTES);
        load_half(An, base);
        asm volatile("s_waitcnt lgkmcnt(8)" ::: "memory");
        load_half(Bn, base + 16384);
      } else {
        asm volatile("s_waitcnt lgkmcnt(0)" ::: "memory");
      }
      __builtin_amdgcn_sched_barrier(0);
      compute(Ac, Bc);
      __builtin_amdgcn_sched_barrier(0);
    };
    WAIT_VM(16);
    __builtin_amdgcn_s_barrier();
    load_half(fa[0], (unsigned)(size_t)smem);
    load_half(fb[0], (unsigned)(size_t)smem + 16384);
    for (int t = 0; t < NT; t += 2) {
      step(t, fa[0], fb[0], fa[1], fb[1]);
      step(t + 1, fa[1], fb[1], fa[0], fb[0]);
    }
    __syncthreads();
    float* red = (float*)smem;
#pragma unroll
    for (int i = 0; i < 4; ++i)
#pragma unroll
      for (int j = 0; j < 4; ++j)
#pragma unroll
        for (int r = 0; r < 4; ++r)
          red[wave * 4352 + (16 * i + (lane >> 4) * 4 + r) * 68 + 16 * j + (lane & 15)] = acc[i][j][r];
    __syncthreads();
    {
      const int row = tid >> 2, c0 = (tid & 3) * 16;
      bf16x8 o[2];
#pragma unroll
      for (int c = 0; c < 16; ++c) {
        const int o_ = row * 68 + c0 + c;
        float v = (red[o_] + red[4352 + o_]) + (red[2 * 4352 + o_] + red[3 * 4352 + o_]);
        float u = rbf(rbf(v) + bf2f(b1[n0 + c0 + c]));
        o[c >> 3][c & 7] = (short)f2bf(gelu_tanh(u));
      }
      bf16x8* dst = (bf16x8*)(G + (size_t)(m0 + row) * 1792 + n0 + c0);
      dst[0] = o[0]; dst[1] = o[1];
    }
    return;
  }
  const int d = blockIdx.x - 224;
  u16* cp = (u16*)smem;
  u16* Khi = (u16*)(smem + 35072);
  u16* Klo = (u16*)(smem + 35072 + 7680);
  float* red = (float*)(smem + 35072 + 15360);
  float* Kd = red;
  {
    bf16x8 uv = *(const bf16x8*)(xnT + (size_t)d * 2048 + tid * 8);
    for (int i = tid; i < 641; i += 256) Kd[i] = Kdg[d * 648 + i];
#pragma unroll
    for (int k = 0; k < 8; ++k)
#pragma unroll
      for (int e = 0; e < 8; ++e) cp[k * CP_STRIDE + 2048 + k - (tid * 8 + e)] = (u16)uv[e];
    if (tid < 8 * 32) {
      const int k = tid >> 5, xx = 2049 + k + (tid & 31);
      if (xx < 2080) cp[k * CP_STRIDE + xx] = 0;
    }
    __syncthreads();
    bf16x8 kh, kl;
#pragma unroll
    for (int e = 0; e < 8; ++e) {
      float kv = Kd[idx_of_l(tid * 8 + e)];
      u16 hv = f2bf(kv);
      kh[e] = (short)hv;
      kl[e] = (short)f2bf(kv - bf2f(hv));
    }
    const int c = (tid >> 2) + 32, w8 = (tid & 3) * 8;
    *(bf16x8*)(Khi + c * 40 + w8) = kh;
    *(bf16x8*)(Klo + c * 40 + w8) = kl;
    if (tid < 128) {
      const int c2 = tid >> 2;
      bf16x8 z = {};
      *(bf16x8*)(Khi + c2 * 40 + w8) = z;
      *(bf16x8*)(Klo + c2 * 40 + w8) = z;
    }
  }
  __syncthreads();
  f32x16 acc0 = {}, acc1 = {};
  {
    const int r = lane & 31, hh = lane >> 5;
    const u16* abase = cp + (r & 7) * CP_STRIDE + 2048 - 8 * (r >> 3) + 8 * hh;
    const int i = lane & 31;
    for (int m = wave; m < 64; m += 4) {
      bf16x8 a0 = *(const bf16x8*)(abase - 32 * m);
      bf16x8 a1 = *(const bf16x8*)(abase - 32 * m + 16);
      {
        const int ch = (32 + i - m + 32) * 40 + 8 * hh;
        bf16x8 bh0 = *(const bf16x8*)(Khi + ch), bh1 = *(const bf16x8*)(Khi + ch + 16);
        bf16x8 bl0 = *(const bf16x8*)(Klo + ch), bl1 = *(const bf16x8*)(Klo + ch + 16);
        acc1 = __builtin_amdgcn_mfma_f32_32x32x16_bf16(a0, bh0, acc1, 0, 0, 0);
        acc1 = __builtin_amdgcn_mfma_f32_32x32x16_bf16(a1, bh1, acc1, 0, 0, 0);
        acc1 = __builtin_amdgcn_mfma_f32_32x32x16_bf16(a0, bl0, acc1, 0, 0, 0);
        acc1 = __builtin_amdgcn_mfma_f32_32x32x16_bf16(a1, bl1, acc1, 0, 0, 0);
      }
      if (m < 32) {
        const int ch = (i - m + 32) * 40 + 8 * hh;
        bf16x8 bh0 = *(const bf16x8*)(Khi + ch), bh1 = *(const bf16x8*)(Khi + ch + 16);
        bf16x8 bl0 = *(const bf16x8*)(Klo + ch), bl1 = *(const bf16x8*)(Klo + ch + 16);
        acc0 = __builtin_amdgcn_mfma_f32_32x32x16_bf16(a0, bh0, acc0, 0, 0, 0);
        acc0 = __builtin_amdgcn_mfma_f32_32x32x16_bf16(a1, bh1, acc0, 0, 0, 0);
        acc0 = __builtin_amdgcn_mfma_f32_32x32x16_bf16(a0, bl0, acc0, 0, 0, 0);
        acc0 = __builtin_amdgcn_mfma_f32_32x32x16_bf16(a1, bl1, acc0, 0, 0, 0);
      }
    }
  }
  {
    const int i = lane & 31, hh = lane >> 5;
#pragma unroll
    for (int rg = 0; rg < 16; ++rg) {
      const int row = (rg & 3) + 8 * (rg >> 2) + 4 * hh;
      red[wave * 2112 + i * 33 + row] = acc0[rg];
      red[wave * 2112 + (32 + i) * 33 + row] = acc1[rg];
    }
  }
  __syncthreads();
  {
    bf16x8 o;
#pragma unroll
    for (int e = 0; e < 8; ++e) {
      const int l = tid * 8 + e;
      const int oo = (l >> 5) * 33 + (l & 31);
      float v = (red[oo] + red[2112 + oo]) + (red[2 * 2112 + oo] + red[3 * 2112 + oo]);
      o[e] = (short)f2bf(v);
    }
    *(bf16x8*)(yT + (size_t)d * 2048 + tid * 8) = o;
  }
}

__global__ __launch_bounds__(256) void gemm2_kernel(const u16* __restrict__ w2, const u16* __restrict__ G,
                                                    const u16* __restrict__ b2, const u16* __restrict__ x,
                                                    const u16* __restrict__ xn, const u16* __restrict__ yT,
                                                    const u16* __restrict__ dskip, u16* __restrict__ out) {
  __shared__ __attribute__((aligned(16))) char smem[NS * STAGE_BYTES];
  const int tid = threadIdx.x, wave = tid >> 6, lane = tid & 63;
  const int bn = (blockIdx.x >> 3) & 7, bm = (blockIdx.x & 7) * 4 + (blockIdx.x >> 6);
  const int m0 = bm * 64, n0 = bn * 64;
  f32x4 acc[4][4] = {};
  const u16* srcA[4]; const u16* srcB[4];
#pragma unroll
  for (int i = 0; i < 4; ++i) {
    const unsigned row = wave * 32 + i * 8 + (lane >> 3);
    srcA[i] = w2 + (size_t)row * 2048 + m0 + kslow_src_chunk(row, lane & 7) * 8;
    const unsigned nr = row & 63, kh = row >> 6;
    srcB[i] = G + (size_t)(n0 + nr) * 1792 + kh * 64 + (((lane & 7) ^ (nr & 7)) * 8);
  }
  auto issue = [&](int t) {
    char* st = smem + (t % NS) * STAGE_BYTES;
#pragma unroll
    for (int i = 0; i < 4; ++i) {
      GLDS(srcA[i] + (size_t)t * 128 * 2048, st + (wave * 32 + i * 8) * 128);
      GLDS(srcB[i] + t * 128, st + 16384 + (wave * 32 + i * 8) * 128);
    }
  };
  const int NT = 14;
#pragma unroll
  for (int s = 0; s < NS - 1; ++s) issue(s);
  const unsigned g = lane >> 4, q = (lane & 15) >> 2, p = lane & 3;
  const unsigned r0 = wave * 32 + 8 * g + q;
  struct F8 { bf16x4 v[8]; };
  struct FB { bf16x8 v[4]; };
  F8 fa[2]; FB fb[2];
  auto loadA = [&](F8& A, unsigned base) {
    tr_read8(base + swz_off(r0, 4 * p), base + swz_off(r0 + 4, 4 * p),
             base + swz_off(r0, 16 + 4 * p), base + swz_off(r0 + 4, 16 + 4 * p), A.v[0], A.v[1], A.v[2], A.v[3]);
    tr_read8(base + swz_off(r0, 32 + 4 * p), base + swz_off(r0 + 4, 32 + 4 * p),
             base + swz_off(r0, 48 + 4 * p), base + swz_off(r0 + 4, 48 + 4 * p), A.v[4], A.v[5], A.v[6], A.v[7]);
  };
  auto loadB = [&](FB& B, unsigned base) {
    const unsigned bb = base + (wave >> 1) * 8192;
    const unsigned ch = (wave & 1) * 4 + (lane >> 4);
    const unsigned n_0 = (lane & 15), n_1 = n_0 + 16, n_2 = n_0 + 32, n_3 = n_0 + 48;
    b128_read2(bb + n_0 * 128 + ((ch ^ (n_0 & 7)) << 4), bb + n_1 * 128 + ((ch ^ (n_1 & 7)) << 4), B.v[0], B.v[1]);
    b128_read2(bb + n_2 * 128 + ((ch ^ (n_2 & 7)) << 4), bb + n_3 * 128 + ((ch ^ (n_3 & 7)) << 4), B.v[2], B.v[3]);
  };
  auto compute = [&](F8& A, FB& B) {
#pragma unroll
    for (int i = 0; i < 4; ++i) {
      bf16x8 a = cat4(A.v[2 * i], A.v[2 * i + 1]);
#pragma unroll
      for (int j = 0; j < 4; ++j)
        acc[i][j] = __builtin_amdgcn_mfma_f32_16x16x32_bf16(a, B.v[j], acc[i][j], 0, 0, 0);
    }
  };
  auto step = [&](int t, F8& Ac, FB& Bc, F8& An, FB& Bn) {
    if (t + NS - 1 <= NT) WAIT_VM(8); else WAIT_VM(0);
    __builtin_amdgcn_s_barrier();
    if (t + NS - 1 < NT) issue(t + NS - 1);
    if (t + 1 < NT) {
      const unsigned base = (unsigned)(size_t)(smem + ((t + 1) % NS) * STAGE_BYTES);
      loadA(An, base);
      loadB(Bn, base + 16384);
      asm volatile("s_waitcnt lgkmcnt(12)" ::: "memory");
    } else {
      asm volatile("s_waitcnt lgkmcnt(0)" ::: "memory");
    }
    __builtin_amdgcn_sched_barrier(0);
    compute(Ac, Bc);
    __builtin_amdgcn_sched_barrier(0);
  };
  WAIT_VM(16);
  __builtin_amdgcn_s_barrier();
  loadA(fa[0], (unsigned)(size_t)smem);
  loadB(fb[0], (unsigned)(size_t)smem + 16384);
  for (int t = 0; t < NT; t += 2) {
    step(t, fa[0], fb[0], fa[1], fb[1]);
    step(t + 1, fa[1], fb[1], fa[0], fb[0]);
  }
  __syncthreads();
  float* red = (float*)smem;
#pragma unroll
  for (int i = 0; i < 4; ++i)
#pragma unroll
    for (int j = 0; j < 4; ++j)
#pragma unroll
      for (int r = 0; r < 4; ++r)
        red[wave * 4352 + (16 * i + (lane >> 4) * 4 + r) * 68 + 16 * j + (lane & 15)] = acc[i][j][r];
  __syncthreads();
  {
    const int row = tid >> 2, c0 = (tid & 3) * 16;
    const int s = m0 + row;
    const float bb2 = bf2f(b2[s]);
    const bf16x8* xp = (const bf16x8*)(x + (size_t)s * 512 + n0 + c0);
    const bf16x8* xnp = (const bf16x8*)(xn + (size_t)s * 512 + n0 + c0);
    const bf16x8* skp = (const bf16x8*)(dskip + n0 + c0);
    bf16x8 xv[2] = {xp[0], xp[1]}, xnv[2] = {xnp[0], xnp[1]}, skv[2] = {skp[0], skp[1]};
    bf16x8 o[2];
#pragma unroll
    for (int c = 0; c < 16; ++c) {
      const int o_ = row * 68 + c0 + c;
      float v = (red[o_] + red[4352 + o_]) + (red[2 * 4352 + o_] + red[3 * 4352 + o_]);
      float val = rbf(rbf(v) + bb2);
      float a = rbf(bf2f(yT[(size_t)(n0 + c0 + c) * 2048 + s]) + rbf(bf2f((u16)xnv[c >> 3][c & 7]) * bf2f((u16)skv[c >> 3][c & 7])));
      float ov = rbf(rbf(bf2f((u16)xv[c >> 3][c & 7]) + a) + val);
      o[c >> 3][c & 7] = (short)f2bf(ov);
    }
    bf16x8* dst = (bf16x8*)(out + (size_t)s * 512 + n0 + c0);
    dst[0] = o[0]; dst[1] = o[1];
  }
}

extern "C" void kernel_launch(void* const* d_in, const int* in_sizes, int n_in,
                              void* d_out, int out_size, void* d_ws, size_t ws_size,
                              hipStream_t stream) {
  const u16* x = (const u16*)d_in[0];
  const u16* lar = (const u16*)d_in[1];
  const u16* aim = (const u16*)d_in[2];
  const u16* cr = (const u16*)d_in[3];
  const u16* ci = (const u16*)d_in[4];
  const u16* logdt = (const u16*)d_in[5];
  const u16* dskip = (const u16*)d_in[6];
  const u16* w1 = (const u16*)d_in[8];
  const u16* b1 = (const u16*)d_in[9];
  const u16* w2 = (const u16*)d_in[10];
  const u16* b2 = (const u16*)d_in[11];
  u16* out = (u16*)d_out;
  char* ws = (char*)d_ws;
  const size_t SZ = (size_t)2048 * 512 * 2;
  u16* xn = (u16*)(ws);
  u16* h = (u16*)(ws + SZ);
  u16* xnT = (u16*)(ws + 2 * SZ);
  u16* yT = (u16*)(ws + 3 * SZ);
  float* Kdg = (float*)(ws + 4 * SZ);
  u16* G = (u16*)(ws + 6 * SZ);
  prep_kernel<<<256 + 2048, 256, 0, stream>>>(x, xn, h, xnT, lar, aim, cr, ci, logdt, Kdg);
  mid_kernel<<<224 + 512, 256, 0, stream>>>(h, w1, b1, G, xnT, Kdg, yT);
  gemm2_kernel<<<256, 256, 0, stream>>>(w2, G, b2, x, xn, yT, dskip, out);
}
```

```cpp
#include <hip/hip_runtime.h>
#include <hip/hip_bf16.h>
#include <stdint.h>

typedef __attribute__((ext_vector_type(8))) short bf16x8;
typedef __attribute__((ext_vector_type(4))) short bf16x4;
typedef __attribute__((ext_vector_type(4))) float f32x4;
typedef __attribute__((ext_vector_type(16))) float f32x16;
typedef unsigned short u16;

__device__ __forceinline__ float bf2f(u16 v) { return __uint_as_float(((unsigned)v) << 16); }
__device__ __forceinline__ u16 f2bf(float f) {
  unsigned u = __float_as_uint(f);
  u += 0x7fffu + ((u >> 16) & 1u);
  return (u16)(u >> 16);
}
__device__ __forceinline__ float rbf(float f) { return bf2f(f2bf(f)); }

__device__ __forceinline__ float wave_sum(float v) {
#pragma unroll
  for (int o = 32; o > 0; o >>= 1) v += __shfl_xor(v, o, 64);
  return v;
}

__device__ __forceinline__ void ln8(const float* x, float* y) {
  float s = 0.f;
#pragma unroll
  for (int i = 0; i < 8; ++i) s += x[i];
  s = wave_sum(s);
  float mean = s * (1.0f / 512.0f);
  float q = 0.f;
#pragma unroll
  for (int i = 0; i < 8; ++i) { float d = x[i] - mean; q += d * d; }
  q = wave_sum(q);
  float var = q * (1.0f / 512.0f);
  float m_b = rbf(mean);
  float v_b = rbf(var);
  float den = rbf(sqrtf(rbf(v_b + 1e-5f)));
#pragma unroll
  for (int i = 0; i < 8; ++i) y[i] = rbf(rbf(x[i] - m_b) / den);
}

__device__ __forceinline__ int idx_of_l(int l) {
  int p = (int)rbf((float)l);
  if (p < 256) return p;
  if (p < 512) return 256 + ((p - 256) >> 1);
  if (p < 1024) return 384 + ((p - 512) >> 2);
  return 512 + ((p - 1024) >> 3);
}
__device__ __forceinline__ float2 cexp_acc(float re, float im) {
  float e = expf(re), s, c;
  sincosf(im, &s, &c);
  return make_float2(e * c, e * s);
}
__device__ __forceinline__ float2 cmul(float2 a, float2 b) {
  return make_float2(a.x * b.x - a.y * b.y, a.x * b.y + a.y * b.x);
}

__global__ __launch_bounds__(256) void prep_kernel(const u16* __restrict__ x, u16* __restrict__ xn,
                                                   u16* __restrict__ h, u16* __restrict__ xnT,
                                                   const u16* __restrict__ lar, const u16* __restrict__ aim,
                                                   const u16* __restrict__ cr, const u16* __restrict__ ci,
                                                   const u16* __restrict__ logdt, float* __restrict__ Kdg) {
  __shared__ __attribute__((aligned(16))) char smem[2 * 16896 + 4096];
  const int tid = threadIdx.x, wave = tid >> 6, lane = tid & 63;
  if (blockIdx.x < 256) {
    u16(*tr)[512 + 8] = (u16(*)[512 + 8])smem;
    const int s0 = blockIdx.x * 8;
    bf16x8 v[2];
#pragma unroll
    for (int rr = 0; rr < 2; ++rr) v[rr] = *(const bf16x8*)(x + (size_t)(s0 + wave * 2 + rr) * 512 + lane * 8);
#pragma unroll
    for (int rr = 0; rr < 2; ++rr) {
      const int r = wave * 2 + rr;
      const int s = s0 + r;
      float xf[8], a[8], b[8];
#pragma unroll
      for (int i = 0; i < 8; ++i) xf[i] = bf2f((u16)v[rr][i]);
      ln8(xf, a);
      ln8(a, b);
      bf16x8 oa, ob;
#pragma unroll
      for (int i = 0; i < 8; ++i) { oa[i] = (short)f2bf(a[i]); ob[i] = (short)f2bf(b[i]); }
      *(bf16x8*)(xn + (size_t)s * 512 + lane * 8) = oa;
      *(bf16x8*)(h + (size_t)s * 512 + lane * 8) = ob;
      *(bf16x8*)(&tr[r][lane * 8]) = oa;
    }
    __syncthreads();
#pragma unroll
    for (int it = 0; it < 2; ++it) {
      const int d = tid + it * 256;
      bf16x8 o;
#pragma unroll
      for (int i = 0; i < 8; ++i) o[i] = (short)tr[i][d];
      *(bf16x8*)(xnT + (size_t)d * 2048 + s0) = o;
    }
    return;
  }
  const int idx = blockIdx.x - 256;
  const int d = idx >> 2, R = idx & 3;
  float(*tabA)[264] = (float(*)[264])smem;
  float(*tabB)[264] = (float(*)[264])(smem + 16896);
  float* red = (float*)(smem + 2 * 16896);
  const float dt = rbf(expf(bf2f(logdt[d])));
  const int n = tid & 127;
  const float ar = -rbf(expf(bf2f(lar[d * 128 + n]))) * dt;
  const float ai = bf2f(aim[d * 128 + n]) * dt;
  const int numA = (R == 0) ? 16 : (R == 3 ? 9 : 8);
  if (tid < 128) {
    const float bstep = (float)(1 << R);
    float2 e = cexp_acc(ar * bstep, ai * bstep);
    float2 p = make_float2(1.f, 0.f);
#pragma unroll
    for (int b = 0; b < 16; ++b) { *(float2*)&tabB[b][2 * n] = p; p = cmul(p, e); }
  } else {
    const float2 Cc = make_float2(bf2f(cr[d * 128 + n]) * dt, bf2f(ci[d * 128 + n]) * dt);
    const float astep = (float)(16 << R);
    const float base = (R == 0) ? 0.f : (float)(128 << R);
    float2 e = cexp_acc(ar * astep, ai * astep);
    float2 p = cmul(Cc, cexp_acc(ar * base, ai * base));
#pragma unroll
    for (int a = 0; a < 16; ++a) {
      *(float2*)&tabA[a][2 * n] = (a < numA) ? make_float2(p.x, -p.y) : make_float2(0.f, 0.f);
      p = (a < numA) ? cmul(p, e) : p;
    }
  }
  __syncthreads();
  {
    f32x4 acc = {};
    const int i = lane & 15, kq = lane >> 4;
#pragma unroll
    for (int st = 0; st < 16; ++st) {
      const int k = wave * 64 + st * 4 + kq;
      acc = __builtin_amdgcn_mfma_f32_16x16x4f32(tabA[i][k], tabB[i][k], acc, 0, 0, 0);
    }
#pragma unroll
    for (int r = 0; r < 4; ++r) red[wave * 256 + ((lane >> 4) * 4 + r) * 16 + (lane & 15)] = acc[r];
  }
  __syncthreads();
  const int nout = (R == 0) ? 256 : (R == 3 ? 129 : 128);
  if (tid < nout) {
    const int off = (R == 0) ? 0 : 128 + 128 * R;
    Kdg[d * 648 + off + tid] = 2.0f * ((red[tid] + red[256 + tid]) + (red[512 + tid] + red[768 + tid]));
  }
}

__device__ __forceinline__ unsigned kslow_src_chunk(unsigned row, unsigned c16) {
  unsigned f = ((row >> 1) & 1) | (((row >> 3) & 1) << 1);
  return ((((c16 >> 1) ^ f)) << 1) | (c16 & 1);
}
__device__ __forceinline__ unsigned swz_off(unsigned row, unsigned col) {
  unsigned c32 = col >> 4;
  unsigned f = ((row >> 1) & 1) | (((row >> 3) & 1) << 1);
  return row * 128 + ((c32 ^ f) << 5) + ((col & 15) << 1);
}
__device__ __forceinline__ float gelu_tanh(float x) {
  float k = 0.7978845608028654f * (x + 0.044715f * x * x * x);
  return 0.5f * x * (1.0f + tanhf(k));
}
#define GLDS(src, dst) __builtin_amdgcn_global_load_lds((const unsigned*)(src), (unsigned*)(dst), 16, 0, 0)
#define NS 4
#define STAGE_BYTES 32768
#define WAIT_VM(n) asm volatile("s_waitcnt vmcnt(" #n ")" ::: "memory")

__device__ __forceinline__ void tr_read8(unsigned a0, unsigned a1, unsigned a2, unsigned a3,
                                         bf16x4& r0, bf16x4& r1, bf16x4& r2, bf16x4& r3) {
  asm volatile("ds_read_b64_tr_b16 %0, %4\n\tds_read_b64_tr_b16 %1, %5\n\tds_read_b64_tr_b16 %2, %6\n\tds_read_b64_tr_b16 %3, %7"
               : "=&v"(r0), "=&v"(r1), "=&v"(r2), "=&v"(r3) : "v"(a0), "v"(a1), "v"(a2), "v"(a3) : "memory");
}

__device__ __forceinline__ void b128_read2(unsigned a0, unsigned a1, bf16x8& r0, bf16x8& r1) {
  asm volatile("ds_read_b128 %0, %2\n\tds_read_b128 %1, %3" : "=&v"(r0), "=&v"(r1) : "v"(a0), "v"(a1) : "memory");
}
struct FragA { bf16x4 v[2][4]; };
__device__ __forceinline__ bf16x8 cat4(bf16x4 a, bf16x4 b) { return bf16x8{a[0], a[1], a[2], a[3], b[0], b[1], b[2], b[3]}; }

#define CP_STRIDE 2192

__global__ __launch_bounds__(256) void mid_kernel(const u16* __restrict__ h, const u16* __restrict__ w1,
                                                  const u16* __restrict__ b1, u16* __restrict__ G,
                                                  int dummy) {
  __shared__ __attribute__((aligned(16))) char smem[NS * STAGE_BYTES];
  const int tid = threadIdx.x, wave = tid >> 6, lane = tid & 63;
  {
    const int tile = (blockIdx.x & 7) * 28 + (blockIdx.x >> 3);
    const int bm = tile & 7, bn = tile >> 3;
    const int m0 = bm * 64, n0 = bn * 64;
    f32x4 acc[4][4] = {};
    const u16* srcA[4]; const u16* srcB[4];
#pragma unroll
    for (int i = 0; i < 4; ++i) {
      const unsigned row = wave * 32 + i * 8 + (lane >> 3);
      const unsigned sc = kslow_src_chunk(row, lane & 7);
      srcA[i] = h + (size_t)row * 512 + m0 + sc * 8;
      srcB[i] = w1 + (size_t)row * 1792 + n0 + sc * 8;
    }
    auto issue = [&](int t) {
      char* st = smem + (t % NS) * STAGE_BYTES;
#pragma unroll
      for (int i = 0; i < 4; ++i) {
        GLDS(srcA[i] + (size_t)t * 128 * 512, st + (wave * 32 + i * 8) * 128);
        GLDS(srcB[i] + (size_t)t * 128 * 1792, st + 16384 + (wave * 32 + i * 8) * 128);
      }
    };
    const int NT = 16;
#pragma unroll
    for (int s = 0; s < NS - 1; ++s) issue(s);
    const unsigned g = lane >> 4, q = (lane & 15) >> 2, p = lane & 3;
    const unsigned r0 = wave * 32 + 8 * g + q;
    struct F8 { bf16x4 v[8]; };
    F8 fa[2], fb[2];
    auto load_half = [&](F8& A, unsigned base) {
      tr_read8(base + swz_off(r0, 4 * p), base + swz_off(r0 + 4, 4 * p),
               base + swz_off(r0, 16 + 4 * p), base + swz_off(r0 + 4, 16 + 4 * p), A.v[0], A.v[1], A.v[2], A.v[3]);
      tr_read8(base + swz_off(r0, 32 + 4 * p), base + swz_off(r0 + 4, 32 + 4 * p),
               base + swz_off(r0, 48 + 4 * p), base + swz_off(r0 + 4, 48 + 4 * p), A.v[4], A.v[5], A.v[6], A.v[7]);
    };
    auto compute = [&](F8& A, F8& B) {
#pragma unroll
      for (int i = 0; i < 4; ++i) {
        bf16x8 a = cat4(A.v[2 * i], A.v[2 * i + 1]);
#pragma unroll
        for (int j = 0; j < 4; ++j)
          acc[i][j] = __builtin_amdgcn_mfma_f32_16x16x32_bf16(a, cat4(B.v[2 * j], B.v[2 * j + 1]), acc[i][j], 0, 0, 0);
      }
    };
    auto step = [&](int t, F8& Ac, F8& Bc, F8& An, F8& Bn) {
      if (t + NS - 1 <= NT) WAIT_VM(8); else WAIT_VM(0);
      __builtin_amdgcn_s_barrier();
      if (t + NS - 1 < NT) issue(t + NS - 1);
      if (t + 1 < NT) {
        const unsigned base = (unsigned)(size_t)(smem + ((t + 1) % NS) * STAGE_BYTES);
        load_half(An, base);
        asm volatile("s_waitcnt lgkmcnt(8)" ::: "memory");
        load_half(Bn, base + 16384);
      } else {
        asm volatile("s_waitcnt lgkmcnt(0)" ::: "memory");
      }
      __builtin_amdgcn_sched_barrier(0);
      compute(Ac, Bc);
      __builtin_amdgcn_sched_barrier(0);
    };
    WAIT_VM(16);
    __builtin_amdgcn_s_barrier();
    load_half(fa[0], (unsigned)(size_t)smem);
    load_half(fb[0], (unsigned)(size_t)smem + 16384);
    for (int t = 0; t < NT; t += 2) {
      step(t, fa[0], fb[0], fa[1], fb[1]);
      step(t + 1, fa[1], fb[1], fa[0], fb[0]);
    }
    __syncthreads();
    float* red = (float*)smem;
#pragma unroll
    for (int i = 0; i < 4; ++i)
#pragma unroll
      for (int j = 0; j < 4; ++j)
#pragma unroll
        for (int r = 0; r < 4; ++r)
          red[wave * 4352 + (16 * i + (lane >> 4) * 4 + r) * 68 + 16 * j + (lane & 15)] = acc[i][j][r];
    __syncthreads();
    {
      const int row = tid >> 2, c0 = (tid & 3) * 16;
      bf16x8 o[2];
#pragma unroll
      for (int c = 0; c < 16; ++c) {
        const int o_ = row * 68 + c0 + c;
        float v = (red[o_] + red[4352 + o_]) + (red[2 * 4352 + o_] + red[3 * 4352 + o_]);
        float u = rbf(rbf(v) + bf2f(b1[n0 + c0 + c]));
        o[c >> 3][c & 7] = (short)f2bf(gelu_tanh(u));
      }
      bf16x8* dst = (bf16x8*)(G + (size_t)(m0 + row) * 1792 + n0 + c0);
      dst[0] = o[0]; dst[1] = o[1];
    }
  }
}

__global__ __launch_bounds__(256) void conv_kernel(const u16* __restrict__ xnT, const float* __restrict__ Kdg,
                                                   u16* __restrict__ yT) {
  __shared__ __attribute__((aligned(16))) char smem[35072 + 15360 + 2 * 2112 * 4];
  const int tid = threadIdx.x, wave = tid >> 6, lane = tid & 63;
  const int d = blockIdx.x;
  u16* cp = (u16*)smem;
  u16* Khi = (u16*)(smem + 35072);
  u16* Klo = (u16*)(smem + 35072 + 7680);
  float* red = (float*)(smem + 35072 + 15360);
  float* Kd = red;
  {
    bf16x8 uv = *(const bf16x8*)(xnT + (size_t)d * 2048 + tid * 8);
    for (int i = tid; i < 641; i += 256) Kd[i] = Kdg[d * 648 + i];
#pragma unroll
    for (int k = 0; k < 8; ++k)
#pragma unroll
      for (int e = 0; e < 8; ++e) cp[k * CP_STRIDE + 2048 + k - (tid * 8 + e)] = (u16)uv[e];
    if (tid < 8 * 32) {
      const int k = tid >> 5, xx = 2049 + k + (tid & 31);
      if (xx < 2080) cp[k * CP_STRIDE + xx] = 0;
    }
    __syncthreads();
    bf16x8 kh, kl;
#pragma unroll
    for (int e = 0; e < 8; ++e) {
      float kv = Kd[idx_of_l(tid * 8 + e)];
      u16 hv = f2bf(kv);
      kh[e] = (short)hv;
      kl[e] = (short)f2bf(kv - bf2f(hv));
    }
    const int c = (tid >> 2) + 32, w8 = (tid & 3) * 8;
    *(bf16x8*)(Khi + c * 40 + w8) = kh;
    *(bf16x8*)(Klo + c * 40 + w8) = kl;
    if (tid < 128) {
      const int c2 = tid >> 2;
      bf16x8 z = {};
      *(bf16x8*)(Khi + c2 * 40 + w8) = z;
      *(bf16x8*)(Klo + c2 * 40 + w8) = z;
    }
  }
  __syncthreads();
  f32x16 acc0 = {}, acc1 = {};
  {
    const int r = lane & 31, hh = lane >> 5;
    const u16* abase = cp + (r & 7) * CP_STRIDE + 2048 - 8 * (r >> 3) + 8 * hh;
    const int i = lane & 31;
    for (int m = wave; m < 64; m += 4) {
      bf16x8 a0 = *(const bf16x8*)(abase - 32 * m);
      bf16x8 a1 = *(const bf16x8*)(abase - 32 * m + 16);
      {
        const int ch = (32 + i - m + 32) * 40 + 8 * hh;
        bf16x8 bh0 = *(const bf16x8*)(Khi + ch), bh1 = *(const bf16x8*)(Khi + ch + 16);
        bf16x8 bl0 = *(const bf16x8*)(Klo + ch), bl1 = *(const bf16x8*)(Klo + ch + 16);
        acc1 = __builtin_amdgcn_mfma_f32_32x32x16_bf16(a0, bh0, acc1, 0, 0, 0);
        acc1 = __builtin_amdgcn_mfma_f32_32x32x16_bf16(a1, bh1, acc1, 0, 0, 0);
        acc1 = __builtin_amdgcn_mfma_f32_32x32x16_bf16(a0, bl0, acc1, 0, 0, 0);
        acc1 = __builtin_amdgcn_mfma_f32_32x32x16_bf16(a1, bl1, acc1, 0, 0, 0);
      }
      if (m < 32) {
        const int ch = (i - m + 32) * 40 + 8 * hh;
        bf16x8 bh0 = *(const bf16x8*)(Khi + ch), bh1 = *(const bf16x8*)(Khi + ch + 16);
        bf16x8 bl0 = *(const bf16x8*)(Klo + ch), bl1 = *(const bf16x8*)(Klo + ch + 16);
        acc0 = __builtin_amdgcn_mfma_f32_32x32x16_bf16(a0, bh0, acc0, 0, 0, 0);
        acc0 = __builtin_amdgcn_mfma_f32_32x32x16_bf16(a1, bh1, acc0, 0, 0, 0);
        acc0 = __builtin_amdgcn_mfma_f32_32x32x16_bf16(a0, bl0, acc0, 0, 0, 0);
        acc0 = __builtin_amdgcn_mfma_f32_32x32x16_bf16(a1, bl1, acc0, 0, 0, 0);
      }
    }
  }
  {
    const int i = lane & 31, hh = lane >> 5;
    if (wave < 2) {
#pragma unroll
      for (int rg = 0; rg < 16; ++rg) {
        const int row = (rg & 3) + 8 * (rg >> 2) + 4 * hh;
        red[wave * 2112 + i * 33 + row] = acc0[rg];
        red[wave * 2112 + (32 + i) * 33 + row] = acc1[rg];
      }
    }
    __syncthreads();
    if (wave >= 2) {
#pragma unroll
      for (int rg = 0; rg < 16; ++rg) {
        const int row = (rg & 3) + 8 * (rg >> 2) + 4 * hh;
        red[(wave - 2) * 2112 + i * 33 + row] += acc0[rg];
        red[(wave - 2) * 2112 + (32 + i) * 33 + row] += acc1[rg];
      }
    }
  }
  __syncthreads();
  {
    bf16x8 o;
#pragma unroll
    for (int e = 0; e < 8; ++e) {
      const int l = tid * 8 + e;
      const int oo = (l >> 5) * 33 + (l & 31);
      o[e] = (short)f2bf(red[oo] + red[2112 + oo]);
    }
    *(bf16x8*)(yT + (size_t)d * 2048 + tid * 8) = o;
  }
}

__global__ __launch_bounds__(256) void gemm2_kernel(const u16* __restrict__ w2, const u16* __restrict__ G,
                                                    const u16* __restrict__ b2, const u16* __restrict__ x,
                                                    const u16* __restrict__ xn, const u16* __restrict__ yT,
                                                    const u16* __restrict__ dskip, u16* __restrict__ out) {
  __shared__ __attribute__((aligned(16))) char smem[NS * STAGE_BYTES];
  const int tid = threadIdx.x, wave = tid >> 6, lane = tid & 63;
  const int bn = (blockIdx.x >> 3) & 7, bm = (blockIdx.x & 7) * 4 + (blockIdx.x >> 6);
  const int m0 = bm * 64, n0 = bn * 64;
  f32x4 acc[4][4] = {};
  const u16* srcA[4]; const u16* srcB[4];
#pragma unroll
  for (int i = 0; i < 4; ++i) {
    const unsigned row = wave * 32 + i * 8 + (lane >> 3);
    srcA[i] = w2 + (size_t)row * 2048 + m0 + kslow_src_chunk(row, lane & 7) * 8;
    const unsigned nr = row & 63, kh = row >> 6;
    srcB[i] = G + (size_t)(n0 + nr) * 1792 + kh * 64 + (((lane & 7) ^ (nr & 7)) * 8);
  }
  auto issue = [&](int t) {
    char* st = smem + (t % NS) * STAGE_BYTES;
#pragma unroll
    for (int i = 0; i < 4; ++i) {
      GLDS(srcA[i] + (size_t)t * 128 * 2048, st + (wave * 32 + i * 8) * 128);
      GLDS(srcB[i] + t * 128, st + 16384 + (wave * 32 + i * 8) * 128);
    }
  };
  const int NT = 14;
#pragma unroll
  for (int s = 0; s < NS - 1; ++s) issue(s);
  const unsigned g = lane >> 4, q = (lane & 15) >> 2, p = lane & 3;
  const unsigned r0 = wave * 32 + 8 * g + q;
  struct F8 { bf16x4 v[8]; };
  struct FB { bf16x8 v[4]; };
  F8 fa[2]; FB fb[2];
  auto loadA = [&](F8& A, unsigned base) {
    tr_read8(base + swz_off(r0, 4 * p), base + swz_off(r0 + 4, 4 * p),
             base + swz_off(r0, 16 + 4 * p), base + swz_off(r0 + 4, 16 + 4 * p), A.v[0], A.v[1], A.v[2], A.v[3]);
    tr_read8(base + swz_off(r0, 32 + 4 * p), base + swz_off(r0 + 4, 32 + 4 * p),
             base + swz_off(r0, 48 + 4 * p), base + swz_off(r0 + 4, 48 + 4 * p), A.v[4], A.v[5], A.v[6], A.v[7]);
  };
  auto loadB = [&](FB& B, unsigned base) {
    const unsigned bb = base + (wave >> 1) * 8192;
    const unsigned ch = (wave & 1) * 4 + (lane >> 4);
    const unsigned n_0 = (lane & 15), n_1 = n_0 + 16, n_2 = n_0 + 32, n_3 = n_0 + 48;
    b128_read2(bb + n_0 * 128 + ((ch ^ (n_0 & 7)) << 4), bb + n_1 * 128 + ((ch ^ (n_1 & 7)) << 4), B.v[0], B.v[1]);
    b128_read2(bb + n_2 * 128 + ((ch ^ (n_2 & 7)) << 4), bb + n_3 * 128 + ((ch ^ (n_3 & 7)) << 4), B.v[2], B.v[3]);
  };
  auto compute = [&](F8& A, FB& B) {
#pragma unroll
    for (int i = 0; i < 4; ++i) {
      bf16x8 a = cat4(A.v[2 * i], A.v[2 * i + 1]);
#pragma unroll
      for (int j = 0; j < 4; ++j)
        acc[i][j] = __builtin_amdgcn_mfma_f32_16x16x32_bf16(a, B.v[j], acc[i][j], 0, 0, 0);
    }
  };
  auto step = [&](int t, F8& Ac, FB& Bc, F8& An, FB& Bn) {
    if (t + NS - 1 <= NT) WAIT_VM(8); else WAIT_VM(0);
    __builtin_amdgcn_s_barrier();
    if (t + NS - 1 < NT) issue(t + NS - 1);
    if (t + 1 < NT) {
      const unsigned base = (unsigned)(size_t)(smem + ((t + 1) % NS) * STAGE_BYTES);
      loadA(An, base);
      loadB(Bn, base + 16384);
      asm volatile("s_waitcnt lgkmcnt(12)" ::: "memory");
    } else {
      asm volatile("s_waitcnt lgkmcnt(0)" ::: "memory");
    }
    __builtin_amdgcn_sched_barrier(0);
    compute(Ac, Bc);
    __builtin_amdgcn_sched_barrier(0);
  };
  WAIT_VM(16);
  __builtin_amdgcn_s_barrier();
  loadA(fa[0], (unsigned)(size_t)smem);
  loadB(fb[0], (unsigned)(size_t)smem + 16384);
  for (int t = 0; t < NT; t += 2) {
    step(t, fa[0], fb[0], fa[1], fb[1]);
    step(t + 1, fa[1], fb[1], fa[0], fb[0]);
  }
  __syncthreads();
  float* red = (float*)smem;
#pragma unroll
  for (int i = 0; i < 4; ++i)
#pragma unroll
    for (int j = 0; j < 4; ++j)
#pragma unroll
      for (int r = 0; r < 4; ++r)
        red[wave * 4352 + (16 * i + (lane >> 4) * 4 + r) * 68 + 16 * j + (lane & 15)] = acc[i][j][r];
  __syncthreads();
  {
    const int row = tid >> 2, c0 = (tid & 3) * 16;
    const int s = m0 + row;
    const float bb2 = bf2f(b2[s]);
    const bf16x8* xp = (const bf16x8*)(x + (size_t)s * 512 + n0 + c0);
    const bf16x8* xnp = (const bf16x8*)(xn + (size_t)s * 512 + n0 + c0);
    const bf16x8* skp = (const bf16x8*)(dskip + n0 + c0);
    bf16x8 xv[2] = {xp[0], xp[1]}, xnv[2] = {xnp[0], xnp[1]}, skv[2] = {skp[0], skp[1]};
    bf16x8 o[2];
#pragma unroll
    for (int c = 0; c < 16; ++c) {
      const int o_ = row * 68 + c0 + c;
      float v = (red[o_] + red[4352 + o_]) + (red[2 * 4352 + o_] + red[3 * 4352 + o_]);
      float val = rbf(rbf(v) + bb2);
      float a = rbf(bf2f(yT[(size_t)(n0 + c0 + c) * 2048 + s]) + rbf(bf2f((u16)xnv[c >> 3][c & 7]) * bf2f((u16)skv[c >> 3][c & 7])));
      float ov = rbf(rbf(bf2f((u16)xv[c >> 3][c & 7]) + a) + val);
      o[c >> 3][c & 7] = (short)f2bf(ov);
    }
    bf16x8* dst = (bf16x8*)(out + (size_t)s * 512 + n0 + c0);
    dst[0] = o[0]; dst[1] = o[1];
  }
}

extern "C" void kernel_launch(void* const* d_in, const int* in_sizes, int n_in,
                              void* d_out, int out_size, void* d_ws, size_t ws_size,
                              hipStream_t stream) {
  const u16* x = (const u16*)d_in[0];
  const u16* lar = (const u16*)d_in[1];
  const u16* aim = (const u16*)d_in[2];
  const u16* cr = (const u16*)d_in[3];
  const u16* ci = (const u16*)d_in[4];
  const u16* logdt = (const u16*)d_in[5];
  const u16* dskip = (const u16*)d_in[6];
  const u16* w1 = (const u16*)d_in[8];
  const u16* b1 = (const u16*)d_in[9];
  const u16* w2 = (const u16*)d_in[10];
  const u16* b2 = (const u16*)d_in[11];
  u16* out = (u16*)d_out;
  char* ws = (char*)d_ws;
  const size_t SZ = (size_t)2048 * 512 * 2;
  u16* xn = (u16*)(ws);
  u16* h = (u16*)(ws + SZ);
  u16* xnT = (u16*)(ws + 2 * SZ);
  u16* yT = (u16*)(ws + 3 * SZ);
  float* Kdg = (float*)(ws + 4 * SZ);
  u16* G = (u16*)(ws + 6 * SZ);
  prep_kernel<<<256 + 2048, 256, 0, stream>>>(x, xn, h, xnT, lar, aim, cr, ci, logdt, Kdg);
  conv_kernel<<<512, 256, 0, stream>>>(xnT, Kdg, yT);
  mid_kernel<<<224, 256, 0, stream>>>(h, w1, b1, G, 0);
  gemm2_kernel<<<256, 256, 0, stream>>>(w2, G, b2, x, xn, yT, dskip, out);
}
```

```cpp
#include <hip/hip_runtime.h>
#include <hip/hip_bf16.h>
#include <stdint.h>

typedef __attribute__((ext_vector_type(8))) short bf16x8;
typedef __attribute__((ext_vector_type(4))) short bf16x4;
typedef __attribute__((ext_vector_type(4))) float f32x4;
typedef __attribute__((ext_vector_type(16))) float f32x16;
typedef unsigned short u16;

__device__ __forceinline__ float bf2f(u16 v) { return __uint_as_float(((unsigned)v) << 16); }
__device__ __forceinline__ u16 f2bf(float f) {
  unsigned u = __float_as_uint(f);
  u += 0x7fffu + ((u >> 16) & 1u);
  return (u16)(u >> 16);
}
__device__ __forceinline__ float rbf(float f) { return bf2f(f2bf(f)); }

__device__ __forceinline__ float wave_sum(float v) {
#pragma unroll
  for (int o = 32; o > 0; o >>= 1) v += __shfl_xor(v, o, 64);
  return v;
}

__device__ __forceinline__ void ln8(const float* x, float* y) {
  float s = 0.f;
#pragma unroll
  for (int i = 0; i < 8; ++i) s += x[i];
  s = wave_sum(s);
  float mean = s * (1.0f / 512.0f);
  float q = 0.f;
#pragma unroll
  for (int i = 0; i < 8; ++i) { float d = x[i] - mean; q += d * d; }
  q = wave_sum(q);
  float var = q * (1.0f / 512.0f);
  float m_b = rbf(mean);
  float v_b = rbf(var);
  float den = rbf(sqrtf(rbf(v_b + 1e-5f)));
#pragma unroll
  for (int i = 0; i < 8; ++i) y[i] = rbf(rbf(x[i] - m_b) / den);
}

__device__ __forceinline__ int idx_of_l(int l) {
  int p = (int)rbf((float)l);
  if (p < 256) return p;
  if (p < 512) return 256 + ((p - 256) >> 1);
  if (p < 1024) return 384 + ((p - 512) >> 2);
  return 512 + ((p - 1024) >> 3);
}
__device__ __forceinline__ float2 cexp_acc(float re, float im) {
  float e = expf(re), s, c;
  sincosf(im, &s, &c);
  return make_float2(e * c, e * s);
}
__device__ __forceinline__ float2 cmul(float2 a, float2 b) {
  return make_float2(a.x * b.x - a.y * b.y, a.x * b.y + a.y * b.x);
}

__global__ __launch_bounds__(256) void prep_kernel(const u16* __restrict__ x, u16* __restrict__ xn,
                                                   u16* __restrict__ h, u16* __restrict__ xnT,
                                                   const u16* __restrict__ lar, const u16* __restrict__ aim,
                                                   const u16* __restrict__ cr, const u16* __restrict__ ci,
                                                   const u16* __restrict__ logdt, float* __restrict__ Kdg) {
  __shared__ __attribute__((aligned(16))) char smem[2 * 16896 + 4096];
  const int tid = threadIdx.x, wave = tid >> 6, lane = tid & 63;
  if (blockIdx.x < 256) {
    u16(*tr)[512 + 8] = (u16(*)[512 + 8])smem;
    const int s0 = blockIdx.x * 8;
    bf16x8 v[2];
#pragma unroll
    for (int rr = 0; rr < 2; ++rr) v[rr] = *(const bf16x8*)(x + (size_t)(s0 + wave * 2 + rr) * 512 + lane * 8);
#pragma unroll
    for (int rr = 0; rr < 2; ++rr) {
      const int r = wave * 2 + rr;
      const int s = s0 + r;
      float xf[8], a[8], b[8];
#pragma unroll
      for (int i = 0; i < 8; ++i) xf[i] = bf2f((u16)v[rr][i]);
      ln8(xf, a);
      ln8(a, b);
      bf16x8 oa, ob;
#pragma unroll
      for (int i = 0; i < 8; ++i) { oa[i] = (short)f2bf(a[i]); ob[i] = (short)f2bf(b[i]); }
      *(bf16x8*)(xn + (size_t)s * 512 + lane * 8) = oa;
      *(bf16x8*)(h + (size_t)s * 512 + lane * 8) = ob;
      *(bf16x8*)(&tr[r][lane * 8]) = oa;
    }
    __syncthreads();
#pragma unroll
    for (int it = 0; it < 2; ++it) {
      const int d = tid + it * 256;
      bf16x8 o;
#pragma unroll
      for (int i = 0; i < 8; ++i) o[i] = (short)tr[i][d];
      *(bf16x8*)(xnT + (size_t)d * 2048 + s0) = o;
    }
    return;
  }
  const int idx = blockIdx.x - 256;
  const int d = idx >> 2, R = idx & 3;
  float(*tabA)[264] = (float(*)[264])smem;
  float(*tabB)[264] = (float(*)[264])(smem + 16896);
  float* red = (float*)(smem + 2 * 16896);
  const float dt = rbf(expf(bf2f(logdt[d])));
  const int n = tid & 127;
  const float ar = -rbf(expf(bf2f(lar[d * 128 + n]))) * dt;
  const float ai = bf2f(aim[d * 128 + n]) * dt;
  const int numA = (R == 0) ? 16 : (R == 3 ? 9 : 8);
  if (tid < 128) {
    const float bstep = (float)(1 << R);
    float2 e = cexp_acc(ar * bstep, ai * bstep);
    float2 p = make_float2(1.f, 0.f);
#pragma unroll
    for (int b = 0; b < 16; ++b) { *(float2*)&tabB[b][2 * n] = p; p = cmul(p, e); }
  } else {
    const float2 Cc = make_float2(bf2f(cr[d * 128 + n]) * dt, bf2f(ci[d * 128 + n]) * dt);
    const float astep = (float)(16 << R);
    const float base = (R == 0) ? 0.f : (float)(128 << R);
    float2 e = cexp_acc(ar * astep, ai * astep);
    float2 p = cmul(Cc, cexp_acc(ar * base, ai * base));
#pragma unroll
    for (int a = 0; a < 16; ++a) {
      *(float2*)&tabA[a][2 * n] = (a < numA) ? make_float2(p.x, -p.y) : make_float2(0.f, 0.f);
      p = (a < numA) ? cmul(p, e) : p;
    }
  }
  __syncthreads();
  {
    f32x4 acc = {};
    const int i = lane & 15, kq = lane >> 4;
#pragma unroll
    for (int st = 0; st < 16; ++st) {
      const int k = wave * 64 + st * 4 + kq;
      acc = __builtin_amdgcn_mfma_f32_16x16x4f32(tabA[i][k], tabB[i][k], acc, 0, 0, 0);
    }
#pragma unroll
    for (int r = 0; r < 4; ++r) red[wave * 256 + ((lane >> 4) * 4 + r) * 16 + (lane & 15)] = acc[r];
  }
  __syncthreads();
  const int nout = (R == 0) ? 256 : (R == 3 ? 129 : 128);
  if (tid < nout) {
    const int off = (R == 0) ? 0 : 128 + 128 * R;
    Kdg[d * 648 + off + tid] = 2.0f * ((red[tid] + red[256 + tid]) + (red[512 + tid] + red[768 + tid]));
  }
}

__device__ __forceinline__ unsigned kslow_src_chunk(unsigned row, unsigned c16) {
  unsigned f = ((row >> 1) & 1) | (((row >> 3) & 1) << 1);
  return ((((c16 >> 1) ^ f)) << 1) | (c16 & 1);
}
__device__ __forceinline__ unsigned swz_off(unsigned row, unsigned col) {
  unsigned c32 = col >> 4;
  unsigned f = ((row >> 1) & 1) | (((row >> 3) & 1) << 1);
  return row * 128 + ((c32 ^ f) << 5) + ((col & 15) << 1);
}
__device__ __forceinline__ float gelu_tanh(float x) {
  float k = 0.7978845608028654f * (x + 0.044715f * x * x * x);
  return 0.5f * x * (1.0f + tanhf(k));
}
#define GLDS(src, dst) __builtin_amdgcn_global_load_lds((const unsigned*)(src), (unsigned*)(dst), 16, 0, 0)
#define NS 4
#define STAGE_BYTES 32768
#define WAIT_VM(n) asm volatile("s_waitcnt vmcnt(" #n ")" ::: "memory")

__device__ __forceinline__ void tr_read8(unsigned a0, unsigned a1, unsigned a2, unsigned a3,
                                         bf16x4& r0, bf16x4& r1, bf16x4& r2, bf16x4& r3) {
  asm volatile("ds_read_b64_tr_b16 %0, %4\n\tds_read_b64_tr_b16 %1, %5\n\tds_read_b64_tr_b16 %2, %6\n\tds_read_b64_tr_b16 %3, %7"
               : "=&v"(r0), "=&v"(r1), "=&v"(r2), "=&v"(r3) : "v"(a0), "v"(a1), "v"(a2), "v"(a3) : "memory");
}

__device__ __forceinline__ void b128_read2(unsigned a0, unsigned a1, bf16x8& r0, bf16x8& r1) {
  asm volatile("ds_read_b128 %0, %2\n\tds_read_b128 %1, %3" : "=&v"(r0), "=&v"(r1) : "v"(a0), "v"(a1) : "memory");
}
struct FragA { bf16x4 v[2][4]; };
__device__ __forceinline__ bf16x8 cat4(bf16x4 a, bf16x4 b) { return bf16x8{a[0], a[1], a[2], a[3], b[0], b[1], b[2], b[3]}; }

#define CP_STRIDE 2192

__global__ __launch_bounds__(256) void mid_kernel(const u16* __restrict__ h, const u16* __restrict__ w1,
                                                  const u16* __restrict__ b1, u16* __restrict__ G,
                                                  int dummy) {
  __shared__ __attribute__((aligned(16))) char smem[NS * STAGE_BYTES];
  const int tid = threadIdx.x, wave = tid >> 6, lane = tid & 63;
  {
    const int tile = (blockIdx.x & 7) * 28 + (blockIdx.x >> 3);
    const int bm = tile & 7, bn = tile >> 3;
    const int m0 = bm * 64, n0 = bn * 64;
    f32x4 acc[4][4] = {};
    const u16* srcA[4]; const u16* srcB[4];
#pragma unroll
    for (int i = 0; i < 4; ++i) {
      const unsigned row = wave * 32 + i * 8 + (lane >> 3);
      const unsigned sc = kslow_src_chunk(row, lane & 7);
      srcA[i] = h + (size_t)row * 512 + m0 + sc * 8;
      srcB[i] = w1 + (size_t)row * 1792 + n0 + sc * 8;
    }
    auto issue = [&](int t) {
      char* st = smem + (t % NS) * STAGE_BYTES;
#pragma unroll
      for (int i = 0; i < 4; ++i) {
        GLDS(srcA[i] + (size_t)t * 128 * 512, st + (wave * 32 + i * 8) * 128);
        GLDS(srcB[i] + (size_t)t * 128 * 1792, st + 16384 + (wave * 32 + i * 8) * 128);
      }
    };
    const int NT = 16;
#pragma unroll
    for (int s = 0; s < NS - 1; ++s) issue(s);
    const unsigned g = lane >> 4, q = (lane & 15) >> 2, p = lane & 3;
    const unsigned r0 = wave * 32 + 8 * g + q;
    struct F8 { bf16x4 v[8]; };
    F8 fa[2], fb[2];
    auto load_half = [&](F8& A, unsigned base) {
      tr_read8(base + swz_off(r0, 4 * p), base + swz_off(r0 + 4, 4 * p),
               base + swz_off(r0, 16 + 4 * p), base + swz_off(r0 + 4, 16 + 4 * p), A.v[0], A.v[1], A.v[2], A.v[3]);
      tr_read8(base + swz_off(r0, 32 + 4 * p), base + swz_off(r0 + 4, 32 + 4 * p),
               base + swz_off(r0, 48 + 4 * p), base + swz_off(r0 + 4, 48 + 4 * p), A.v[4], A.v[5], A.v[6], A.v[7]);
    };
    auto compute = [&](F8& A, F8& B) {
#pragma unroll
      for (int i = 0; i < 4; ++i) {
        bf16x8 a = cat4(A.v[2 * i], A.v[2 * i + 1]);
#pragma unroll
        for (int j = 0; j < 4; ++j)
          acc[i][j] = __builtin_amdgcn_mfma_f32_16x16x32_bf16(a, cat4(B.v[2 * j], B.v[2 * j + 1]), acc[i][j], 0, 0, 0);
      }
    };
    auto step = [&](int t, F8& Ac, F8& Bc, F8& An, F8& Bn) {
      if (t + NS - 1 <= NT) WAIT_VM(8); else WAIT_VM(0);
      __builtin_amdgcn_s_barrier();
      if (t + NS - 1 < NT) issue(t + NS - 1);
      if (t + 1 < NT) {
        const unsigned base = (unsigned)(size_t)(smem + ((t + 1) % NS) * STAGE_BYTES);
        load_half(An, base);
        asm volatile("s_waitcnt lgkmcnt(8)" ::: "memory");
        load_half(Bn, base + 16384);
      } else {
        asm volatile("s_waitcnt lgkmcnt(0)" ::: "memory");
      }
      __builtin_amdgcn_sched_barrier(0);
      compute(Ac, Bc);
      __builtin_amdgcn_sched_barrier(0);
    };
    WAIT_VM(16);
    __builtin_amdgcn_s_barrier();
    load_half(fa[0], (unsigned)(size_t)smem);
    load_half(fb[0], (unsigned)(size_t)smem + 16384);
    for (int t = 0; t < NT; t += 2) {
      step(t, fa[0], fb[0], fa[1], fb[1]);
      step(t + 1, fa[1], fb[1], fa[0], fb[0]);
    }
    __syncthreads();
    float* red = (float*)smem;
#pragma unroll
    for (int i = 0; i < 4; ++i)
#pragma unroll
      for (int j = 0; j < 4; ++j)
#pragma unroll
        for (int r = 0; r < 4; ++r)
          red[wave * 4352 + (16 * i + (lane >> 4) * 4 + r) * 68 + 16 * j + (lane & 15)] = acc[i][j][r];
    __syncthreads();
    {
      const int row = tid >> 2, c0 = (tid & 3) * 16;
      bf16x8 o[2];
#pragma unroll
      for (int c = 0; c < 16; ++c) {
        const int o_ = row * 68 + c0 + c;
        float v = (red[o_] + red[4352 + o_]) + (red[2 * 4352 + o_] + red[3 * 4352 + o_]);
        float u = rbf(rbf(v) + bf2f(b1[n0 + c0 + c]));
        o[c >> 3][c & 7] = (short)f2bf(gelu_tanh(u));
      }
      bf16x8* dst = (bf16x8*)(G + (size_t)(m0 + row) * 1792 + n0 + c0);
      dst[0] = o[0]; dst[1] = o[1];
    }
  }
}

__global__ __launch_bounds__(256) void conv_kernel(const u16* __restrict__ xnT, const float* __restrict__ Kdg,
                                                   u16* __restrict__ yT) {
  __shared__ __attribute__((aligned(16))) char smem[35072 + 15360 + 2 * 2112 * 4];
  const int tid = threadIdx.x, wave = tid >> 6, lane = tid & 63;
  const int d = blockIdx.x;
  u16* cp = (u16*)smem;
  u16* Khi = (u16*)(smem + 35072);
  u16* Klo = (u16*)(smem + 35072 + 7680);
  float* red = (float*)(smem + 35072 + 15360);
  float* Kd = red;
  {
    typedef __attribute__((ext_vector_type(4))) unsigned u32x4;
    const u16* up = xnT + (size_t)d * 2048;
    u32x4 w0 = *(const u32x4*)(up + tid * 8);
    u32x4 w1v = {};
    if (tid < 255) w1v = *(const u32x4*)(up + tid * 8 + 8);
    for (int i = tid; i < 641; i += 256) Kd[i] = Kdg[d * 648 + i];
    unsigned dw[8] = {w0[0], w0[1], w0[2], w0[3], w1v[0], w1v[1], w1v[2], w1v[3]};
#pragma unroll
    for (int k = 0; k < 8; ++k) {
      u32x4 o;
#pragma unroll
      for (int j = 0; j < 4; ++j)
        o[j] = (k & 1) ? __builtin_amdgcn_alignbyte(dw[j + (k + 1) / 2], dw[j + (k - 1) / 2], 2) : dw[j + k / 2];
      *(u32x4*)(cp + k * CP_STRIDE + 32 + tid * 8) = o;
    }
    if (tid < 32 * 8) {
      const int k = tid >> 5, xx = (tid & 31) - 32;
      const int ui = xx + k;
      cp[k * CP_STRIDE + 32 + xx] = (ui >= 0) ? up[ui] : (u16)0;
    }
    __syncthreads();
    bf16x8 kh, kl;
#pragma unroll
    for (int e = 0; e < 8; ++e) {
      const int l = (tid >> 2) * 32 + 31 - ((tid & 3) * 8 + e);
      float kv = Kd[idx_of_l(l)];
      u16 hv = f2bf(kv);
      kh[e] = (short)hv;
      kl[e] = (short)f2bf(kv - bf2f(hv));
    }
    const int c = (tid >> 2) + 32, w8 = (tid & 3) * 8;
    *(bf16x8*)(Khi + c * 40 + w8) = kh;
    *(bf16x8*)(Klo + c * 40 + w8) = kl;
    if (tid < 128) {
      const int c2 = tid >> 2;
      bf16x8 z = {};
      *(bf16x8*)(Khi + c2 * 40 + w8) = z;
      *(bf16x8*)(Klo + c2 * 40 + w8) = z;
    }
  }
  __syncthreads();
  f32x16 acc0 = {}, acc1 = {};
  {
    const int r = lane & 31, hh = lane >> 5;
    const u16* abase = cp + ((r + 1) & 7) * CP_STRIDE + 8 * ((r + 1) >> 3) + 8 * hh;
    const int i = lane & 31;
    for (int m = wave; m < 64; m += 4) {
      bf16x8 a0 = *(const bf16x8*)(abase + 32 * m);
      bf16x8 a1 = *(const bf16x8*)(abase + 32 * m + 16);
      {
        const int ch = (32 + i - m + 32) * 40 + 8 * hh;
        bf16x8 bh0 = *(const bf16x8*)(Khi + ch), bh1 = *(const bf16x8*)(Khi + ch + 16);
        bf16x8 bl0 = *(const bf16x8*)(Klo + ch), bl1 = *(const bf16x8*)(Klo + ch + 16);
        acc1 = __builtin_amdgcn_mfma_f32_32x32x16_bf16(a0, bh0, acc1, 0, 0, 0);
        acc1 = __builtin_amdgcn_mfma_f32_32x32x16_bf16(a1, bh1, acc1, 0, 0, 0);
        acc1 = __builtin_amdgcn_mfma_f32_32x32x16_bf16(a0, bl0, acc1, 0, 0, 0);
        acc1 = __builtin_amdgcn_mfma_f32_32x32x16_bf16(a1, bl1, acc1, 0, 0, 0);
      }
      if (m < 32) {
        const int ch = (i - m + 32) * 40 + 8 * hh;
        bf16x8 bh0 = *(const bf16x8*)(Khi + ch), bh1 = *(const bf16x8*)(Khi + ch + 16);
        bf16x8 bl0 = *(const bf16x8*)(Klo + ch), bl1 = *(const bf16x8*)(Klo + ch + 16);
        acc0 = __builtin_amdgcn_mfma_f32_32x32x16_bf16(a0, bh0, acc0, 0, 0, 0);
        acc0 = __builtin_amdgcn_mfma_f32_32x32x16_bf16(a1, bh1, acc0, 0, 0, 0);
        acc0 = __builtin_amdgcn_mfma_f32_32x32x16_bf16(a0, bl0, acc0, 0, 0, 0);
        acc0 = __builtin_amdgcn_mfma_f32_32x32x16_bf16(a1, bl1, acc0, 0, 0, 0);
      }
    }
  }
  {
    const int i = lane & 31, hh = lane >> 5;
    if (wave < 2) {
#pragma unroll
      for (int rg = 0; rg < 16; ++rg) {
        const int row = (rg & 3) + 8 * (rg >> 2) + 4 * hh;
        red[wave * 2112 + i * 33 + row] = acc0[rg];
        red[wave * 2112 + (32 + i) * 33 + row] = acc1[rg];
      }
    }
    __syncthreads();
    if (wave >= 2) {
#pragma unroll
      for (int rg = 0; rg < 16; ++rg) {
        const int row = (rg & 3) + 8 * (rg >> 2) + 4 * hh;
        red[(wave - 2) * 2112 + i * 33 + row] += acc0[rg];
        red[(wave - 2) * 2112 + (32 + i) * 33 + row] += acc1[rg];
      }
    }
  }
  __syncthreads();
  {
    bf16x8 o;
#pragma unroll
    for (int e = 0; e < 8; ++e) {
      const int l = tid * 8 + e;
      const int oo = (l >> 5) * 33 + (l & 31);
      o[e] = (short)f2bf(red[oo] + red[2112 + oo]);
    }
    *(bf16x8*)(yT + (size_t)d * 2048 + tid * 8) = o;
  }
}

__global__ __launch_bounds__(256) void gemm2_kernel(const u16* __restrict__ w2, const u16* __restrict__ G,
                                                    const u16* __restrict__ b2, const u16* __restrict__ x,
                                                    const u16* __restrict__ xn, const u16* __restrict__ yT,
                                                    const u16* __restrict__ dskip, u16* __restrict__ out) {
  __shared__ __attribute__((aligned(16))) char smem[NS * STAGE_BYTES];
  const int tid = threadIdx.x, wave = tid >> 6, lane = tid & 63;
  const int bn = (blockIdx.x >> 3) & 7, bm = (blockIdx.x & 7) * 4 + (blockIdx.x >> 6);
  const int m0 = bm * 64, n0 = bn * 64;
  f32x4 acc[4][4] = {};
  const u16* srcA[4]; const u16* srcB[4];
#pragma unroll
  for (int i = 0; i < 4; ++i) {
    const unsigned row = wave * 32 + i * 8 + (lane >> 3);
    srcA[i] = w2 + (size_t)row * 2048 + m0 + kslow_src_chunk(row, lane & 7) * 8;
    const unsigned nr = row & 63, kh = row >> 6;
    srcB[i] = G + (size_t)(n0 + nr) * 1792 + kh * 64 + (((lane & 7) ^ (nr & 7)) * 8);
  }
  auto issue = [&](int t) {
    char* st = smem + (t % NS) * STAGE_BYTES;
#pragma unroll
    for (int i = 0; i < 4; ++i) {
      GLDS(srcA[i] + (size_t)t * 128 * 2048, st + (wave * 32 + i * 8) * 128);
      GLDS(srcB[i] + t * 128, st + 16384 + (wave * 32 + i * 8) * 128);
    }
  };
  const int NT = 14;
#pragma unroll
  for (int s = 0; s < NS - 1; ++s) issue(s);
  const unsigned g = lane >> 4, q = (lane & 15) >> 2, p = lane & 3;
  const unsigned r0 = wave * 32 + 8 * g + q;
  struct F8 { bf16x4 v[8]; };
  struct FB { bf16x8 v[4]; };
  F8 fa[2]; FB fb[2];
  auto loadA = [&](F8& A, unsigned base) {
    tr_read8(base + swz_off(r0, 4 * p), base + swz_off(r0 + 4, 4 * p),
             base + swz_off(r0, 16 + 4 * p), base + swz_off(r0 + 4, 16 + 4 * p), A.v[0], A.v[1], A.v[2], A.v[3]);
    tr_read8(base + swz_off(r0, 32 + 4 * p), base + swz_off(r0 + 4, 32 + 4 * p),
             base + swz_off(r0, 48 + 4 * p), base + swz_off(r0 + 4, 48 + 4 * p), A.v[4], A.v[5], A.v[6], A.v[7]);
  };
  auto loadB = [&](FB& B, unsigned base) {
    const unsigned bb = base + (wave >> 1) * 8192;
    const unsigned ch = (wave & 1) * 4 + (lane >> 4);
    const unsigned n_0 = (lane & 15), n_1 = n_0 + 16, n_2 = n_0 + 32, n_3 = n_0 + 48;
    b128_read2(bb + n_0 * 128 + ((ch ^ (n_0 & 7)) << 4), bb + n_1 * 128 + ((ch ^ (n_1 & 7)) << 4), B.v[0], B.v[1]);
    b128_read2(bb + n_2 * 128 + ((ch ^ (n_2 & 7)) << 4), bb + n_3 * 128 + ((ch ^ (n_3 & 7)) << 4), B.v[2], B.v[3]);
  };
  auto compute = [&](F8& A, FB& B) {
#pragma unroll
    for (int i = 0; i < 4; ++i) {
      bf16x8 a = cat4(A.v[2 * i], A.v[2 * i + 1]);
#pragma unroll
      for (int j = 0; j < 4; ++j)
        acc[i][j] = __builtin_amdgcn_mfma_f32_16x16x32_bf16(a, B.v[j], acc[i][j], 0, 0, 0);
    }
  };
  auto step = [&](int t, F8& Ac, FB& Bc, F8& An, FB& Bn) {
    if (t + NS - 1 <= NT) WAIT_VM(8); else WAIT_VM(0);
    __builtin_amdgcn_s_barrier();
    if (t + NS - 1 < NT) issue(t + NS - 1);
    if (t + 1 < NT) {
      const unsigned base = (unsigned)(size_t)(smem + ((t + 1) % NS) * STAGE_BYTES);
      loadA(An, base);
      loadB(Bn, base + 16384);
      asm volatile("s_waitcnt lgkmcnt(12)" ::: "memory");
    } else {
      asm volatile("s_waitcnt lgkmcnt(0)" ::: "memory");
    }
    __builtin_amdgcn_sched_barrier(0);
    compute(Ac, Bc);
    __builtin_amdgcn_sched_barrier(0);
  };
  WAIT_VM(16);
  __builtin_amdgcn_s_barrier();
  loadA(fa[0], (unsigned)(size_t)smem);
  loadB(fb[0], (unsigned)(size_t)smem + 16384);
  for (int t = 0; t < NT; t += 2) {
    step(t, fa[0], fb[0], fa[1], fb[1]);
    step(t + 1, fa[1], fb[1], fa[0], fb[0]);
  }
  __syncthreads();
  float* red = (float*)smem;
#pragma unroll
  for (int i = 0; i < 4; ++i)
#pragma unroll
    for (int j = 0; j < 4; ++j)
#pragma unroll
      for (int r = 0; r < 4; ++r)
        red[wave * 4352 + (16 * i + (lane >> 4) * 4 + r) * 68 + 16 * j + (lane & 15)] = acc[i][j][r];
  __syncthreads();
  {
    const int row = tid >> 2, c0 = (tid & 3) * 16;
    const int s = m0 + row;
    const float bb2 = bf2f(b2[s]);
    const bf16x8* xp = (const bf16x8*)(x + (size_t)s * 512 + n0 + c0);
    const bf16x8* xnp = (const bf16x8*)(xn + (size_t)s * 512 + n0 + c0);
    const bf16x8* skp = (const bf16x8*)(dskip + n0 + c0);
    bf16x8 xv[2] = {xp[0], xp[1]}, xnv[2] = {xnp[0], xnp[1]}, skv[2] = {skp[0], skp[1]};
    bf16x8 o[2];
#pragma unroll
    for (int c = 0; c < 16; ++c) {
      const int o_ = row * 68 + c0 + c;
      float v = (red[o_] + red[4352 + o_]) + (red[2 * 4352 + o_] + red[3 * 4352 + o_]);
      float val = rbf(rbf(v) + bb2);
      float a = rbf(bf2f(yT[(size_t)(n0 + c0 + c) * 2048 + s]) + rbf(bf2f((u16)xnv[c >> 3][c & 7]) * bf2f((u16)skv[c >> 3][c & 7])));
      float ov = rbf(rbf(bf2f((u16)xv[c >> 3][c & 7]) + a) + val);
      o[c >> 3][c & 7] = (short)f2bf(ov);
    }
    bf16x8* dst = (bf16x8*)(out + (size_t)s * 512 + n0 + c0);
    dst[0] = o[0]; dst[1] = o[1];
  }
}

extern "C" void kernel_launch(void* const* d_in, const int* in_sizes, int n_in,
                              void* d_out, int out_size, void* d_ws, size_t ws_size,
                              hipStream_t stream) {
  const u16* x = (const u16*)d_in[0];
  const u16* lar = (const u16*)d_in[1];
  const u16* aim = (const u16*)d_in[2];
  const u16* cr = (const u16*)d_in[3];
  const u16* ci = (const u16*)d_in[4];
  const u16* logdt = (const u16*)d_in[5];
  const u16* dskip = (const u16*)d_in[6];
  const u16* w1 = (const u16*)d_in[8];
  const u16* b1 = (const u16*)d_in[9];
  const u16* w2 = (const u16*)d_in[10];
  const u16* b2 = (const u16*)d_in[11];
  u16* out = (u16*)d_out;
  char* ws = (char*)d_ws;
  const size_t SZ = (size_t)2048 * 512 * 2;
  u16* xn = (u16*)(ws);
  u16* h = (u16*)(ws + SZ);
  u16* xnT = (u16*)(ws + 2 * SZ);
  u16* yT = (u16*)(ws + 3 * SZ);
  float* Kdg = (float*)(ws + 4 * SZ);
  u16* G = (u16*)(ws + 6 * SZ);
  prep_kernel<<<256 + 2048, 256, 0, stream>>>(x, xn, h, xnT, lar, aim, cr, ci, logdt, Kdg);
  conv_kernel<<<512, 256, 0, stream>>>(xnT, Kdg, yT);
  mid_kernel<<<224, 256, 0, stream>>>(h, w1, b1, G, 0);
  gemm2_kernel<<<256, 256, 0, stream>>>(w2, G, b2, x, xn, yT, dskip, out);
}
```

```cpp
#include <hip/hip_runtime.h>
#include <hip/hip_bf16.h>
#include <stdint.h>

typedef __attribute__((ext_vector_type(8))) short bf16x8;
typedef __attribute__((ext_vector_type(4))) short bf16x4;
typedef __attribute__((ext_vector_type(4))) float f32x4;
typedef __attribute__((ext_vector_type(16))) float f32x16;
typedef unsigned short u16;

__device__ __forceinline__ float bf2f(u16 v) { return __uint_as_float(((unsigned)v) << 16); }
__device__ __forceinline__ u16 f2bf(float f) {
  unsigned u = __float_as_uint(f);
  u += 0x7fffu + ((u >> 16) & 1u);
  return (u16)(u >> 16);
}
__device__ __forceinline__ float rbf(float f) { return bf2f(f2bf(f)); }

__device__ __forceinline__ float wave_sum(float v) {
#pragma unroll
  for (int o = 32; o > 0; o >>= 1) v += __shfl_xor(v, o, 64);
  return v;
}

__device__ __forceinline__ void ln8(const float* x, float* y) {
  float s = 0.f;
#pragma unroll
  for (int i = 0; i < 8; ++i) s += x[i];
  s = wave_sum(s);
  float mean = s * (1.0f / 512.0f);
  float q = 0.f;
#pragma unroll
  for (int i = 0; i < 8; ++i) { float d = x[i] - mean; q += d * d; }
  q = wave_sum(q);
  float var = q * (1.0f / 512.0f);
  float m_b = rbf(mean);
  float v_b = rbf(var);
  float den = rbf(sqrtf(rbf(v_b + 1e-5f)));
#pragma unroll
  for (int i = 0; i < 8; ++i) y[i] = rbf(rbf(x[i] - m_b) / den);
}

__device__ __forceinline__ int idx_of_l(int l) {
  int p = (int)rbf((float)l);
  if (p < 256) return p;
  if (p < 512) return 256 + ((p - 256) >> 1);
  if (p < 1024) return 384 + ((p - 512) >> 2);
  return 512 + ((p - 1024) >> 3);
}
__device__ __forceinline__ float2 cexp_acc(float re, float im) {
  float e = expf(re), s, c;
  sincosf(im, &s, &c);
  return make_float2(e * c, e * s);
}
__device__ __forceinline__ float2 cmul(float2 a, float2 b) {
  return make_float2(a.x * b.x - a.y * b.y, a.x * b.y + a.y * b.x);
}

__global__ __launch_bounds__(256) void prep_kernel(const u16* __restrict__ x, u16* __restrict__ xn,
                                                   u16* __restrict__ h, u16* __restrict__ xnT,
                                                   const u16* __restrict__ lar, const u16* __restrict__ aim,
                                                   const u16* __restrict__ cr, const u16* __restrict__ ci,
                                                   const u16* __restrict__ logdt, float* __restrict__ Kdg,
                                                   const u16* __restrict__ w1, const u16* __restrict__ w2) {
  __shared__ __attribute__((aligned(16))) char smem[2 * 16896 + 4096];
  const int tid = threadIdx.x, wave = tid >> 6, lane = tid & 63;
  const unsigned bid = (blockIdx.x < 256) ? blockIdx.x + 2304 : blockIdx.x - 256;
  if (bid >= 2304) {
    typedef __attribute__((ext_vector_type(4))) unsigned u32x4;
    const int j = bid - 2304, xc = j & 7, jj = j >> 3;
    u32x4 a[7], b[7];
#pragma unroll
    for (int i = 0; i < 7; ++i) {
      const int c = i * 256 + tid;
      const int r1 = c / 28, c1 = c - r1 * 28;
      a[i] = *(const u32x4*)(w1 + (size_t)(jj * 64 + r1) * 1792 + xc * 224 + c1 * 8);
    }
#pragma unroll
    for (int i = 0; i < 7; ++i) {
      const int c = i * 256 + tid;
      b[i] = *(const u32x4*)(w2 + (size_t)(jj * 56 + (c >> 5)) * 2048 + xc * 256 + (c & 31) * 8);
    }
#pragma unroll
    for (int i = 0; i < 7; ++i) asm volatile("" :: "v"(a[i]), "v"(b[i]));
    return;
  }
  if (bid < 256) {
    u16(*tr)[512 + 8] = (u16(*)[512 + 8])smem;
    const int s0 = bid * 8;
    bf16x8 v[2];
#pragma unroll
    for (int rr = 0; rr < 2; ++rr) v[rr] = *(const bf16x8*)(x + (size_t)(s0 + wave * 2 + rr) * 512 + lane * 8);
#pragma unroll
    for (int rr = 0; rr < 2; ++rr) {
      const int r = wave * 2 + rr;
      const int s = s0 + r;
      float xf[8], a[8], b[8];
#pragma unroll
      for (int i = 0; i < 8; ++i) xf[i] = bf2f((u16)v[rr][i]);
      ln8(xf, a);
      ln8(a, b);
      bf16x8 oa, ob;
#pragma unroll
      for (int i = 0; i < 8; ++i) { oa[i] = (short)f2bf(a[i]); ob[i] = (short)f2bf(b[i]); }
      *(bf16x8*)(xn + (size_t)s * 512 + lane * 8) = oa;
      *(bf16x8*)(h + (size_t)s * 512 + lane * 8) = ob;
      *(bf16x8*)(&tr[r][lane * 8]) = oa;
    }
    __syncthreads();
#pragma unroll
    for (int it = 0; it < 2; ++it) {
      const int d = tid + it * 256;
      bf16x8 o;
#pragma unroll
      for (int i = 0; i < 8; ++i) o[i] = (short)tr[i][d];
      *(bf16x8*)(xnT + (size_t)d * 2048 + s0) = o;
    }
    return;
  }
  const int idx = bid - 256;
  const int d = idx >> 2, R = idx & 3;
  float(*tabA)[264] = (float(*)[264])smem;
  float(*tabB)[264] = (float(*)[264])(smem + 16896);
  float* red = (float*)(smem + 2 * 16896);
  const float dt = rbf(expf(bf2f(logdt[d])));
  const int n = tid & 127;
  const float ar = -rbf(expf(bf2f(lar[d * 128 + n]))) * dt;
  const float ai = bf2f(aim[d * 128 + n]) * dt;
  const int numA = (R == 0) ? 16 : (R == 3 ? 9 : 8);
  if (tid < 128) {
    const float bstep = (float)(1 << R);
    float2 e = cexp_acc(ar * bstep, ai * bstep);
    float2 p = make_float2(1.f, 0.f);
#pragma unroll
    for (int b = 0; b < 16; ++b) { *(float2*)&tabB[b][2 * n] = p; p = cmul(p, e); }
  } else {
    const float2 Cc = make_float2(bf2f(cr[d * 128 + n]) * dt, bf2f(ci[d * 128 + n]) * dt);
    const float astep = (float)(16 << R);
    const float base = (R == 0) ? 0.f : (float)(128 << R);
    float2 e = cexp_acc(ar * astep, ai * astep);
    float2 p = cmul(Cc, cexp_acc(ar * base, ai * base));
#pragma unroll
    for (int a = 0; a < 16; ++a) {
      *(float2*)&tabA[a][2 * n] = (a < numA) ? make_float2(p.x, -p.y) : make_float2(0.f, 0.f);
      p = (a < numA) ? cmul(p, e) : p;
    }
  }
  __syncthreads();
  {
    f32x4 acc = {};
    const int i = lane & 15, kq = lane >> 4;
#pragma unroll
    for (int st = 0; st < 16; ++st) {
      const int k = wave * 64 + st * 4 + kq;
      acc = __builtin_amdgcn_mfma_f32_16x16x4f32(tabA[i][k], tabB[i][k], acc, 0, 0, 0);
    }
#pragma unroll
    for (int r = 0; r < 4; ++r) red[wave * 256 + ((lane >> 4) * 4 + r) * 16 + (lane & 15)] = acc[r];
  }
  __syncthreads();
  const int nout = (R == 0) ? 256 : (R == 3 ? 129 : 128);
  if (tid < nout) {
    const int off = (R == 0) ? 0 : 128 + 128 * R;
    Kdg[d * 648 + off + tid] = 2.0f * ((red[tid] + red[256 + tid]) + (red[512 + tid] + red[768 + tid]));
  }
}

__device__ __forceinline__ unsigned kslow_src_chunk(unsigned row, unsigned c16) {
  unsigned f = ((row >> 1) & 1) | (((row >> 3) & 1) << 1);
  return ((((c16 >> 1) ^ f)) << 1) | (c16 & 1);
}
__device__ __forceinline__ unsigned swz_off(unsigned row, unsigned col) {
  unsigned c32 = col >> 4;
  unsigned f = ((row >> 1) & 1) | (((row >> 3) & 1) << 1);
  return row * 128 + ((c32 ^ f) << 5) + ((col & 15) << 1);
}
__device__ __forceinline__ float gelu_tanh(float x) {
  float k = 0.7978845608028654f * (x + 0.044715f * x * x * x);
  return 0.5f * x * (1.0f + tanhf(k));
}
#define GLDS(src, dst) __builtin_amdgcn_global_load_lds((const unsigned*)(src), (unsigned*)(dst), 16, 0, 0)
#define NS 4
#define STAGE_BYTES 32768
#define WAIT_VM(n) asm volatile("s_waitcnt vmcnt(" #n ")" ::: "memory")

__device__ __forceinline__ void tr_read8(unsigned a0, unsigned a1, unsigned a2, unsigned a3,
                                         bf16x4& r0, bf16x4& r1, bf16x4& r2, bf16x4& r3) {
  asm volatile("ds_read_b64_tr_b16 %0, %4\n\tds_read_b64_tr_b16 %1, %5\n\tds_read_b64_tr_b16 %2, %6\n\tds_read_b64_tr_b16 %3, %7"
               : "=&v"(r0), "=&v"(r1), "=&v"(r2), "=&v"(r3) : "v"(a0), "v"(a1), "v"(a2), "v"(a3) : "memory");
}

__device__ __forceinline__ void b128_read2(unsigned a0, unsigned a1, bf16x8& r0, bf16x8& r1) {
  asm volatile("ds_read_b128 %0, %2\n\tds_read_b128 %1, %3" : "=&v"(r0), "=&v"(r1) : "v"(a0), "v"(a1) : "memory");
}
struct FragA { bf16x4 v[2][4]; };
__device__ __forceinline__ bf16x8 cat4(bf16x4 a, bf16x4 b) { return bf16x8{a[0], a[1], a[2], a[3], b[0], b[1], b[2], b[3]}; }

#define CP_STRIDE 2192

__global__ __launch_bounds__(256) void mid_kernel(const u16* __restrict__ h, const u16* __restrict__ w1,
                                                  const u16* __restrict__ b1, u16* __restrict__ G,
                                                  int dummy) {
  __shared__ __attribute__((aligned(16))) char smem[NS * STAGE_BYTES];
  const int tid = threadIdx.x, wave = tid >> 6, lane = tid & 63;
  {
    const int tile = (blockIdx.x & 7) * 28 + (blockIdx.x >> 3);
    const int bm = tile & 7, bn = tile >> 3;
    const int m0 = bm * 64, n0 = bn * 64;
    f32x4 acc[4][4] = {};
    const u16* srcA[4]; const u16* srcB[4];
#pragma unroll
    for (int i = 0; i < 4; ++i) {
      const unsigned row = wave * 32 + i * 8 + (lane >> 3);
      const unsigned sc = kslow_src_chunk(row, lane & 7);
      srcA[i] = h + (size_t)row * 512 + m0 + sc * 8;
      srcB[i] = w1 + (size_t)row * 1792 + n0 + sc * 8;
    }
    auto issue = [&](int t) {
      char* st = smem + (t % NS) * STAGE_BYTES;
#pragma unroll
      for (int i = 0; i < 4; ++i) {
        GLDS(srcA[i] + (size_t)t * 128 * 512, st + (wave * 32 + i * 8) * 128);
        GLDS(srcB[i] + (size_t)t * 128 * 1792, st + 16384 + (wave * 32 + i * 8) * 128);
      }
    };
    const int NT = 16;
#pragma unroll
    for (int s = 0; s < NS - 1; ++s) issue(s);
    const unsigned g = lane >> 4, q = (lane & 15) >> 2, p = lane & 3;
    const unsigned r0 = wave * 32 + 8 * g + q;
    struct F8 { bf16x4 v[8]; };
    F8 fa[2], fb[2];
    auto load_half = [&](F8& A, unsigned base) {
      tr_read8(base + swz_off(r0, 4 * p), base + swz_off(r0 + 4, 4 * p),
               base + swz_off(r0, 16 + 4 * p), base + swz_off(r0 + 4, 16 + 4 * p), A.v[0], A.v[1], A.v[2], A.v[3]);
      tr_read8(base + swz_off(r0, 32 + 4 * p), base + swz_off(r0 + 4, 32 + 4 * p),
               base + swz_off(r0, 48 + 4 * p), base + swz_off(r0 + 4, 48 + 4 * p), A.v[4], A.v[5], A.v[6], A.v[7]);
    };
    auto compute = [&](F8& A, F8& B) {
#pragma unroll
      for (int i = 0; i < 4; ++i) {
        bf16x8 a = cat4(A.v[2 * i], A.v[2 * i + 1]);
#pragma unroll
        for (int j = 0; j < 4; ++j)
          acc[i][j] = __builtin_amdgcn_mfma_f32_16x16x32_bf16(a, cat4(B.v[2 * j], B.v[2 * j + 1]), acc[i][j], 0, 0, 0);
      }
    };
    auto step = [&](int t, F8& Ac, F8& Bc, F8& An, F8& Bn) {
      if (t + NS - 1 <= NT) WAIT_VM(8); else WAIT_VM(0);
      __builtin_amdgcn_s_barrier();
      if (t + NS - 1 < NT) issue(t + NS - 1);
      if (t + 1 < NT) {
        const unsigned base = (unsigned)(size_t)(smem + ((t + 1) % NS) * STAGE_BYTES);
        load_half(An, base);
        asm volatile("s_waitcnt lgkmcnt(8)" ::: "memory");
        load_half(Bn, base + 16384);
      } else {
        asm volatile("s_waitcnt lgkmcnt(0)" ::: "memory");
      }
      __builtin_amdgcn_sched_barrier(0);
      compute(Ac, Bc);
      __builtin_amdgcn_sched_barrier(0);
    };
    WAIT_VM(16);
    __builtin_amdgcn_s_barrier();
    load_half(fa[0], (unsigned)(size_t)smem);
    load_half(fb[0], (unsigned)(size_t)smem + 16384);
    for (int t = 0; t < NT; t += 2) {
      step(t, fa[0], fb[0], fa[1], fb[1]);
      step(t + 1, fa[1], fb[1], fa[0], fb[0]);
    }
    __syncthreads();
    float* red = (float*)smem;
#pragma unroll
    for (int i = 0; i < 4; ++i)
#pragma unroll
      for (int j = 0; j < 4; ++j)
#pragma unroll
        for (int r = 0; r < 4; ++r)
          red[wave * 4352 + (16 * i + (lane >> 4) * 4 + r) * 68 + 16 * j + (lane & 15)] = acc[i][j][r];
    __syncthreads();
    {
      const int row = tid >> 2, c0 = (tid & 3) * 16;
      bf16x8 o[2];
#pragma unroll
      for (int c = 0; c < 16; ++c) {
        const int o_ = row * 68 + c0 + c;
        float v = (red[o_] + red[4352 + o_]) + (red[2 * 4352 + o_] + red[3 * 4352 + o_]);
        float u = rbf(rbf(v) + bf2f(b1[n0 + c0 + c]));
        o[c >> 3][c & 7] = (short)f2bf(gelu_tanh(u));
      }
      bf16x8* dst = (bf16x8*)(G + (size_t)(m0 + row) * 1792 + n0 + c0);
      dst[0] = o[0]; dst[1] = o[1];
    }
  }
}

__global__ __launch_bounds__(256) void conv_kernel(const u16* __restrict__ xnT, const float* __restrict__ Kdg,
                                                   u16* __restrict__ yT) {
  __shared__ __attribute__((aligned(16))) char smem[35072 + 15360 + 2 * 2112 * 4];
  const int tid = threadIdx.x, wave = tid >> 6, lane = tid & 63;
  const int d = blockIdx.x;
  u16* cp = (u16*)smem;
  u16* Khi = (u16*)(smem + 35072);
  u16* Klo = (u16*)(smem + 35072 + 7680);
  float* red = (float*)(smem + 35072 + 15360);
  float* Kd = red;
  {
    typedef __attribute__((ext_vector_type(4))) unsigned u32x4;
    const u16* up = xnT + (size_t)d * 2048;
    u32x4 w0 = *(const u32x4*)(up + tid * 8);
    u32x4 w1v = {};
    if (tid < 255) w1v = *(const u32x4*)(up + tid * 8 + 8);
    for (int i = tid; i < 641; i += 256) Kd[i] = Kdg[d * 648 + i];
    unsigned dw[8] = {w0[0], w0[1], w0[2], w0[3], w1v[0], w1v[1], w1v[2], w1v[3]};
#pragma unroll
    for (int k = 0; k < 8; ++k) {
      u32x4 o;
#pragma unroll
      for (int j = 0; j < 4; ++j)
        o[j] = (k & 1) ? __builtin_amdgcn_alignbyte(dw[j + (k + 1) / 2], dw[j + (k - 1) / 2], 2) : dw[j + k / 2];
      *(u32x4*)(cp + k * CP_STRIDE + 32 + tid * 8) = o;
    }
    if (tid < 32 * 8) {
      const int k = tid >> 5, xx = (tid & 31) - 32;
      const int ui = xx + k;
      cp[k * CP_STRIDE + 32 + xx] = (ui >= 0) ? up[ui] : (u16)0;
    }
    __syncthreads();
    bf16x8 kh, kl;
#pragma unroll
    for (int e = 0; e < 8; ++e) {
      const int l = (tid >> 2) * 32 + 31 - ((tid & 3) * 8 + e);
      float kv = Kd[idx_of_l(l)];
      u16 hv = f2bf(kv);
      kh[e] = (short)hv;
      kl[e] = (short)f2bf(kv - bf2f(hv));
    }
    const int c = (tid >> 2) + 32, w8 = (tid & 3) * 8;
    *(bf16x8*)(Khi + c * 40 + w8) = kh;
    *(bf16x8*)(Klo + c * 40 + w8) = kl;
    if (tid < 128) {
      const int c2 = tid >> 2;
      bf16x8 z = {};
      *(bf16x8*)(Khi + c2 * 40 + w8) = z;
      *(bf16x8*)(Klo + c2 * 40 + w8) = z;
    }
  }
  __syncthreads();
  f32x16 acc0 = {}, acc1 = {};
  {
    const int r = lane & 31, hh = lane >> 5;
    const u16* abase = cp + ((r + 1) & 7) * CP_STRIDE + 8 * ((r + 1) >> 3) + 8 * hh;
    const int i = lane & 31;
    for (int m = wave; m < 64; m += 4) {
      bf16x8 a0 = *(const bf16x8*)(abase + 32 * m);
      bf16x8 a1 = *(const bf16x8*)(abase + 32 * m + 16);
      {
        const int ch = (32 + i - m + 32) * 40 + 8 * hh;
        bf16x8 bh0 = *(const bf16x8*)(Khi + ch), bh1 = *(const bf16x8*)(Khi + ch + 16);
        bf16x8 bl0 = *(const bf16x8*)(Klo + ch), bl1 = *(const bf16x8*)(Klo + ch + 16);
        acc1 = __builtin_amdgcn_mfma_f32_32x32x16_bf16(a0, bh0, acc1, 0, 0, 0);
        acc1 = __builtin_amdgcn_mfma_f32_32x32x16_bf16(a1, bh1, acc1, 0, 0, 0);
        acc1 = __builtin_amdgcn_mfma_f32_32x32x16_bf16(a0, bl0, acc1, 0, 0, 0);
        acc1 = __builtin_amdgcn_mfma_f32_32x32x16_bf16(a1, bl1, acc1, 0, 0, 0);
      }
      if (m < 32) {
        const int ch = (i - m + 32) * 40 + 8 * hh;
        bf16x8 bh0 = *(const bf16x8*)(Khi + ch), bh1 = *(const bf16x8*)(Khi + ch + 16);
        bf16x8 bl0 = *(const bf16x8*)(Klo + ch), bl1 = *(const bf16x8*)(Klo + ch + 16);
        acc0 = __builtin_amdgcn_mfma_f32_32x32x16_bf16(a0, bh0, acc0, 0, 0, 0);
        acc0 = __builtin_amdgcn_mfma_f32_32x32x16_bf16(a1, bh1, acc0, 0, 0, 0);
        acc0 = __builtin_amdgcn_mfma_f32_32x32x16_bf16(a0, bl0, acc0, 0, 0, 0);
        acc0 = __builtin_amdgcn_mfma_f32_32x32x16_bf16(a1, bl1, acc0, 0, 0, 0);
      }
    }
  }
  {
    const int i = lane & 31, hh = lane >> 5;
    if (wave < 2) {
#pragma unroll
      for (int rg = 0; rg < 16; ++rg) {
        const int row = (rg & 3) + 8 * (rg >> 2) + 4 * hh;
        red[wave * 2112 + i * 33 + row] = acc0[rg];
        red[wave * 2112 + (32 + i) * 33 + row] = acc1[rg];
      }
    }
    __syncthreads();
    if (wave >= 2) {
#pragma unroll
      for (int rg = 0; rg < 16; ++rg) {
        const int row = (rg & 3) + 8 * (rg >> 2) + 4 * hh;
        red[(wave - 2) * 2112 + i * 33 + row] += acc0[rg];
        red[(wave - 2) * 2112 + (32 + i) * 33 + row] += acc1[rg];
      }
    }
  }
  __syncthreads();
  {
    bf16x8 o;
#pragma unroll
    for (int e = 0; e < 8; ++e) {
      const int l = tid * 8 + e;
      const int oo = (l >> 5) * 33 + (l & 31);
      o[e] = (short)f2bf(red[oo] + red[2112 + oo]);
    }
    *(bf16x8*)(yT + (size_t)d * 2048 + tid * 8) = o;
  }
}

__global__ __launch_bounds__(256) void gemm2_kernel(const u16* __restrict__ w2, const u16* __restrict__ G,
                                                    const u16* __restrict__ b2, const u16* __restrict__ x,
                                                    const u16* __restrict__ xn, const u16* __restrict__ yT,
                                                    const u16* __restrict__ dskip, u16* __restrict__ out) {
  __shared__ __attribute__((aligned(16))) char smem[NS * STAGE_BYTES];
  const int tid = threadIdx.x, wave = tid >> 6, lane = tid & 63;
  const int bn = (blockIdx.x >> 3) & 7, bm = (blockIdx.x & 7) * 4 + (blockIdx.x >> 6);
  const int m0 = bm * 64, n0 = bn * 64;
  f32x4 acc[4][4] = {};
  const u16* srcA[4]; const u16* srcB[4];
#pragma unroll
  for (int i = 0; i < 4; ++i) {
    const unsigned row = wave * 32 + i * 8 + (lane >> 3);
    srcA[i] = w2 + (size_t)row * 2048 + m0 + kslow_src_chunk(row, lane & 7) * 8;
    const unsigned nr = row & 63, kh = row >> 6;
    srcB[i] = G + (size_t)(n0 + nr) * 1792 + kh * 64 + (((lane & 7) ^ (nr & 7)) * 8);
  }
  auto issue = [&](int t) {
    char* st = smem + (t % NS) * STAGE_BYTES;
#pragma unroll
    for (int i = 0; i < 4; ++i) {
      GLDS(srcA[i] + (size_t)t * 128 * 2048, st + (wave * 32 + i * 8) * 128);
      GLDS(srcB[i] + t * 128, st + 16384 + (wave * 32 + i * 8) * 128);
    }
  };
  const int NT = 14;
#pragma unroll
  for (int s = 0; s < NS - 1; ++s) issue(s);
  const unsigned g = lane >> 4, q = (lane & 15) >> 2, p = lane & 3;
  const unsigned r0 = wave * 32 + 8 * g + q;
  struct F8 { bf16x4 v[8]; };
  struct FB { bf16x8 v[4]; };
  F8 fa[2]; FB fb[2];
  auto loadA = [&](F8& A, unsigned base) {
    tr_read8(base + swz_off(r0, 4 * p), base + swz_off(r0 + 4, 4 * p),
             base + swz_off(r0, 16 + 4 * p), base + swz_off(r0 + 4, 16 + 4 * p), A.v[0], A.v[1], A.v[2], A.v[3]);
    tr_read8(base + swz_off(r0, 32 + 4 * p), base + swz_off(r0 + 4, 32 + 4 * p),
             base + swz_off(r0, 48 + 4 * p), base + swz_off(r0 + 4, 48 + 4 * p), A.v[4], A.v[5], A.v[6], A.v[7]);
  };
  auto loadB = [&](FB& B, unsigned base) {
    const unsigned bb = base + (wave >> 1) * 8192;
    const unsigned ch = (wave & 1) * 4 + (lane >> 4);
    const unsigned n_0 = (lane & 15), n_1 = n_0 + 16, n_2 = n_0 + 32, n_3 = n_0 + 48;
    b128_read2(bb + n_0 * 128 + ((ch ^ (n_0 & 7)) << 4), bb + n_1 * 128 + ((ch ^ (n_1 & 7)) << 4), B.v[0], B.v[1]);
    b128_read2(bb + n_2 * 128 + ((ch ^ (n_2 & 7)) << 4), bb + n_3 * 128 + ((ch ^ (n_3 & 7)) << 4), B.v[2], B.v[3]);
  };
  auto compute = [&](F8& A, FB& B) {
#pragma unroll
    for (int i = 0; i < 4; ++i) {
      bf16x8 a = cat4(A.v[2 * i], A.v[2 * i + 1]);
#pragma unroll
      for (int j = 0; j < 4; ++j)
        acc[i][j] = __builtin_amdgcn_mfma_f32_16x16x32_bf16(a, B.v[j], acc[i][j], 0, 0, 0);
    }
  };
  auto step = [&](int t, F8& Ac, FB& Bc, F8& An, FB& Bn) {
    if (t + NS - 1 <= NT) WAIT_VM(8); else WAIT_VM(0);
    __builtin_amdgcn_s_barrier();
    if (t + NS - 1 < NT) issue(t + NS - 1);
    if (t + 1 < NT) {
      const unsigned base = (unsigned)(size_t)(smem + ((t + 1) % NS) * STAGE_BYTES);
      loadA(An, base);
      loadB(Bn, base + 16384);
      asm volatile("s_waitcnt lgkmcnt(12)" ::: "memory");
    } else {
      asm volatile("s_waitcnt lgkmcnt(0)" ::: "memory");
    }
    __builtin_amdgcn_sched_barrier(0);
    compute(Ac, Bc);
    __builtin_amdgcn_sched_barrier(0);
  };
  WAIT_VM(16);
  __builtin_amdgcn_s_barrier();
  loadA(fa[0], (unsigned)(size_t)smem);
  loadB(fb[0], (unsigned)(size_t)smem + 16384);
  for (int t = 0; t < NT; t += 2) {
    step(t, fa[0], fb[0], fa[1], fb[1]);
    step(t + 1, fa[1], fb[1], fa[0], fb[0]);
  }
  __syncthreads();
  float* red = (float*)smem;
#pragma unroll
  for (int i = 0; i < 4; ++i)
#pragma unroll
    for (int j = 0; j < 4; ++j)
#pragma unroll
      for (int r = 0; r < 4; ++r)
        red[wave * 4352 + (16 * i + (lane >> 4) * 4 + r) * 68 + 16 * j + (lane & 15)] = acc[i][j][r];
  __syncthreads();
  {
    const int row = tid >> 2, c0 = (tid & 3) * 16;
    const int s = m0 + row;
    const float bb2 = bf2f(b2[s]);
    const bf16x8* xp = (const bf16x8*)(x + (size_t)s * 512 + n0 + c0);
    const bf16x8* xnp = (const bf16x8*)(xn + (size_t)s * 512 + n0 + c0);
    const bf16x8* skp = (const bf16x8*)(dskip + n0 + c0);
    bf16x8 xv[2] = {xp[0], xp[1]}, xnv[2] = {xnp[0], xnp[1]}, skv[2] = {skp[0], skp[1]};
    bf16x8 o[2];
#pragma unroll
    for (int c = 0; c < 16; ++c) {
      const int o_ = row * 68 + c0 + c;
      float v = (red[o_] + red[4352 + o_]) + (red[2 * 4352 + o_] + red[3 * 4352 + o_]);
      float val = rbf(rbf(v) + bb2);
      float a = rbf(bf2f(yT[(size_t)(n0 + c0 + c) * 2048 + s]) + rbf(bf2f((u16)xnv[c >> 3][c & 7]) * bf2f((u16)skv[c >> 3][c & 7])));
      float ov = rbf(rbf(bf2f((u16)xv[c >> 3][c & 7]) + a) + val);
      o[c >> 3][c & 7] = (short)f2bf(ov);
    }
    bf16x8* dst = (bf16x8*)(out + (size_t)s * 512 + n0 + c0);
    dst[0] = o[0]; dst[1] = o[1];
  }
}

extern "C" void kernel_launch(void* const* d_in, const int* in_sizes, int n_in,
                              void* d_out, int out_size, void* d_ws, size_t ws_size,
                              hipStream_t stream) {
  const u16* x = (const u16*)d_in[0];
  const u16* lar = (const u16*)d_in[1];
  const u16* aim = (const u16*)d_in[2];
  const u16* cr = (const u16*)d_in[3];
  const u16* ci = (const u16*)d_in[4];
  const u16* logdt = (const u16*)d_in[5];
  const u16* dskip = (const u16*)d_in[6];
  const u16* w1 = (const u16*)d_in[8];
  const u16* b1 = (const u16*)d_in[9];
  const u16* w2 = (const u16*)d_in[10];
  const u16* b2 = (const u16*)d_in[11];
  u16* out = (u16*)d_out;
  char* ws = (char*)d_ws;
  const size_t SZ = (size_t)2048 * 512 * 2;
  u16* xn = (u16*)(ws);
  u16* h = (u16*)(ws + SZ);
  u16* xnT = (u16*)(ws + 2 * SZ);
  u16* yT = (u16*)(ws + 3 * SZ);
  float* Kdg = (float*)(ws + 4 * SZ);
  u16* G = (u16*)(ws + 6 * SZ);
  prep_kernel<<<256 + 2048 + 256, 256, 0, stream>>>(x, xn, h, xnT, lar, aim, cr, ci, logdt, Kdg, w1, w2);
  conv_kernel<<<512, 256, 0, stream>>>(xnT, Kdg, yT);
  mid_kernel<<<224, 256, 0, stream>>>(h, w1, b1, G, 0);
  gemm2_kernel<<<256, 256, 0, stream>>>(w2, G, b2, x, xn, yT, dskip, out);
}
```

```cpp
#include <hip/hip_runtime.h>
#include <hip/hip_bf16.h>
#include <stdint.h>

typedef __attribute__((ext_vector_type(8))) short bf16x8;
typedef __attribute__((ext_vector_type(4))) short bf16x4;
typedef __attribute__((ext_vector_type(4))) float f32x4;
typedef __attribute__((ext_vector_type(16))) float f32x16;
typedef unsigned short u16;

__device__ __forceinline__ float bf2f(u16 v) { return __uint_as_float(((unsigned)v) << 16); }
__device__ __forceinline__ u16 f2bf(float f) {
  unsigned u = __float_as_uint(f);
  u += 0x7fffu + ((u >> 16) & 1u);
  return (u16)(u >> 16);
}
__device__ __forceinline__ float rbf(float f) { return bf2f(f2bf(f)); }

template <int CTRL>
__device__ __forceinline__ float dpp_add(float v) {
  return v + __int_as_float(__builtin_amdgcn_update_dpp(0, __float_as_int(v), CTRL, 0xf, 0xf, true));
}
__device__ __forceinline__ float wave_sum(float v) {
  v = dpp_add<0xB1>(v);
  v = dpp_add<0x4E>(v);
  v = dpp_add<0x141>(v);
  v = dpp_add<0x140>(v);
  const int iv = __float_as_int(v);
  const float a = __int_as_float(__builtin_amdgcn_readlane(iv, 0)), b = __int_as_float(__builtin_amdgcn_readlane(iv, 16));
  const float c = __int_as_float(__builtin_amdgcn_readlane(iv, 32)), d = __int_as_float(__builtin_amdgcn_readlane(iv, 48));
  return (a + b) + (c + d);
}

__device__ __forceinline__ void ln8x2(const float (*x)[8], float (*y)[8]) {
  float s[2], q[2], mean[2];
#pragma unroll
  for (int r = 0; r < 2; ++r) {
    s[r] = ((x[r][0] + x[r][1]) + (x[r][2] + x[r][3])) + ((x[r][4] + x[r][5]) + (x[r][6] + x[r][7]));
  }
#pragma unroll
  for (int r = 0; r < 2; ++r) mean[r] = wave_sum(s[r]) * (1.0f / 512.0f);
#pragma unroll
  for (int r = 0; r < 2; ++r) {
    q[r] = 0.f;
#pragma unroll
    for (int i = 0; i < 8; ++i) { float d = x[r][i] - mean[r]; q[r] += d * d; }
  }
#pragma unroll
  for (int r = 0; r < 2; ++r) {
    float var = wave_sum(q[r]) * (1.0f / 512.0f);
    float m_b = rbf(mean[r]);
    float v_b = rbf(var);
    float den = rbf(sqrtf(rbf(v_b + 1e-5f)));
#pragma unroll
    for (int i = 0; i < 8; ++i) y[r][i] = rbf(rbf(x[r][i] - m_b) / den);
  }
}

__device__ __forceinline__ int idx_of_l(int l) {
  int p = (int)rbf((float)l);
  if (p < 256) return p;
  if (p < 512) return 256 + ((p - 256) >> 1);
  if (p < 1024) return 384 + ((p - 512) >> 2);
  return 512 + ((p - 1024) >> 3);
}
__device__ __forceinline__ float2 cexp_acc(float re, float im) {
  float e = expf(re), s, c;
  sincosf(im, &s, &c);
  return make_float2(e * c, e * s);
}
__device__ __forceinline__ float2 cmul(float2 a, float2 b) {
  return make_float2(a.x * b.x - a.y * b.y, a.x * b.y + a.y * b.x);
}

__global__ __launch_bounds__(256) void prep_kernel(const u16* __restrict__ x, u16* __restrict__ xn,
                                                   u16* __restrict__ h, u16* __restrict__ xnT,
                                                   const u16* __restrict__ lar, const u16* __restrict__ aim,
                                                   const u16* __restrict__ cr, const u16* __restrict__ ci,
                                                   const u16* __restrict__ logdt, float* __restrict__ Kdg,
                                                   const u16* __restrict__ w1, const u16* __restrict__ w2) {
  __shared__ __attribute__((aligned(16))) char smem[4 * 8704];
  const int tid = threadIdx.x, wave = tid >> 6, lane = tid & 63;
  const unsigned bid = blockIdx.x;
  if (bid >= 768) {
    typedef __attribute__((ext_vector_type(4))) unsigned u32x4;
    const int j = bid - 768, xc = j & 7, jj = j >> 3;
    u32x4 a[7], b[7];
#pragma unroll
    for (int i = 0; i < 7; ++i) {
      const int c = i * 256 + tid;
      const int r1 = c / 28, c1 = c - r1 * 28;
      a[i] = *(const u32x4*)(w1 + (size_t)(jj * 64 + r1) * 1792 + xc * 224 + c1 * 8);
    }
#pragma unroll
    for (int i = 0; i < 7; ++i) {
      const int c = i * 256 + tid;
      b[i] = *(const u32x4*)(w2 + (size_t)(jj * 56 + (c >> 5)) * 2048 + xc * 256 + (c & 31) * 8);
    }
#pragma unroll
    for (int i = 0; i < 7; ++i) asm volatile("" :: "v"(a[i]), "v"(b[i]));
    return;
  }
  if (bid < 256) {
    u16(*tr)[512 + 8] = (u16(*)[512 + 8])smem;
    const int s0 = ((bid & 7) * 32 + (bid >> 3)) * 8;
    bf16x8 v[2];
#pragma unroll
    for (int rr = 0; rr < 2; ++rr) v[rr] = *(const bf16x8*)(x + (size_t)(s0 + wave * 2 + rr) * 512 + lane * 8);
    float xf[2][8], a[2][8], b[2][8];
#pragma unroll
    for (int rr = 0; rr < 2; ++rr)
#pragma unroll
      for (int i = 0; i < 8; ++i) xf[rr][i] = bf2f((u16)v[rr][i]);
    ln8x2(xf, a);
    ln8x2(a, b);
#pragma unroll
    for (int rr = 0; rr < 2; ++rr) {
      const int r = wave * 2 + rr;
      const int s = s0 + r;
      bf16x8 oa, ob;
#pragma unroll
      for (int i = 0; i < 8; ++i) { oa[i] = (short)f2bf(a[rr][i]); ob[i] = (short)f2bf(b[rr][i]); }
      *(bf16x8*)(&tr[r][lane * 8]) = oa;
      *(bf16x8*)(xn + (size_t)s * 512 + lane * 8) = oa;
      *(bf16x8*)(h + (size_t)s * 512 + lane * 8) = ob;
    }
    __syncthreads();
#pragma unroll
    for (int it = 0; it < 2; ++it) {
      const int d = tid + it * 256;
      bf16x8 o;
#pragma unroll
      for (int i = 0; i < 8; ++i) o[i] = (short)tr[i][d];
      *(bf16x8*)(xnT + (size_t)d * 2048 + s0) = o;
    }
    return;
  }
  const int d = bid - 256, R = wave;
  float* tab = (float*)(smem + wave * 8704);
  const int half = lane >> 5, m = lane & 31;
  u16 pl[4], pi[4], pr[4], pc[4];
#pragma unroll
  for (int c = 0; c < 4; ++c) {
    const int n = d * 128 + c * 32 + m;
    pl[c] = lar[n]; pi[c] = aim[n]; pr[c] = cr[n]; pc[c] = ci[n];
  }
  const float dt = rbf(expf(bf2f(logdt[d])));
  const int numrows = half ? ((R == 0) ? 16 : (R == 3 ? 9 : 8)) : 16;
  const float step = (float)((half ? 16 : 1) << R);
  const float base = (half && R) ? (float)(128 << R) : 0.f;
  const float sgn = half ? -1.f : 1.f;
  float* wp = tab + half * (16 * 68) + 2 * m;
  const float* rpA = tab + 16 * 68 + (lane & 15) * 68 + (lane >> 4) * 4;
  const float* rpB = tab + (lane & 15) * 68 + (lane >> 4) * 4;
  f32x4 acc = {};
#pragma unroll
  for (int c = 0; c < 4; ++c) {
    const float ar = -rbf(expf(bf2f(pl[c]))) * dt;
    const float ai = bf2f(pi[c]) * dt;
    const float2 Cc = half ? make_float2(bf2f(pr[c]) * dt, bf2f(pc[c]) * dt) : make_float2(1.f, 0.f);
    const float2 e = cexp_acc(ar * step, ai * step);
    float2 p = cmul(Cc, cexp_acc(ar * base, ai * base));
#pragma unroll
    for (int r = 0; r < 16; ++r) {
      *(float2*)(wp + r * 68) = (r < numrows) ? make_float2(p.x, sgn * p.y) : make_float2(0.f, 0.f);
      p = cmul(p, e);
    }
    asm volatile("" ::: "memory");
#pragma unroll
    for (int g = 0; g < 4; ++g) {
      const f32x4 av = *(const f32x4*)(rpA + g * 16);
      const f32x4 bv = *(const f32x4*)(rpB + g * 16);
#pragma unroll
      for (int e2 = 0; e2 < 4; ++e2) acc = __builtin_amdgcn_mfma_f32_16x16x4f32(av[e2], bv[e2], acc, 0, 0, 0);
    }
    asm volatile("" ::: "memory");
  }
  const int nout = (R == 0) ? 256 : (R == 3 ? 129 : 128);
  const int off = (R == 0) ? 0 : 128 + 128 * R;
#pragma unroll
  for (int r = 0; r < 4; ++r) {
    const int idx = ((lane >> 4) * 4 + r) * 16 + (lane & 15);
    if (idx < nout) Kdg[d * 648 + off + idx] = 2.0f * acc[r];
  }
}

__device__ __forceinline__ unsigned kslow_src_chunk(unsigned row, unsigned c16) {
  unsigned f = ((row >> 1) & 1) | (((row >> 3) & 1) << 1);
  return ((((c16 >> 1) ^ f)) << 1) | (c16 & 1);
}
__device__ __forceinline__ unsigned swz_off(unsigned row, unsigned col) {
  unsigned c32 = col >> 4;
  unsigned f = ((row >> 1) & 1) | (((row >> 3) & 1) << 1);
  return row * 128 + ((c32 ^ f) << 5) + ((col & 15) << 1);
}
__device__ __forceinline__ float gelu_tanh(float x) {
  float k = 0.7978845608028654f * (x + 0.044715f * x * x * x);
  return 0.5f * x * (1.0f + tanhf(k));
}
#define GLDS(src, dst) __builtin_amdgcn_global_load_lds((const unsigned*)(src), (unsigned*)(dst), 16, 0, 0)
#define NS 4
#define STAGE_BYTES 32768
#define WAIT_VM(n) asm volatile("s_waitcnt vmcnt(" #n ")" ::: "memory")

__device__ __forceinline__ void tr_read8(unsigned a0, unsigned a1, unsigned a2, unsigned a3,
                                         bf16x4& r0, bf16x4& r1, bf16x4& r2, bf16x4& r3) {
  asm volatile("ds_read_b64_tr_b16 %0, %4\n\tds_read_b64_tr_b16 %1, %5\n\tds_read_b64_tr_b16 %2, %6\n\tds_read_b64_tr_b16 %3, %7"
               : "=&v"(r0), "=&v"(r1), "=&v"(r2), "=&v"(r3) : "v"(a0), "v"(a1), "v"(a2), "v"(a3) : "memory");
}

__device__ __forceinline__ void b128_read2(unsigned a0, unsigned a1, bf16x8& r0, bf16x8& r1) {
  asm volatile("ds_read_b128 %0, %2\n\tds_read_b128 %1, %3" : "=&v"(r0), "=&v"(r1) : "v"(a0), "v"(a1) : "memory");
}
struct FragA { bf16x4 v[2][4]; };
__device__ __forceinline__ bf16x8 cat4(bf16x4 a, bf16x4 b) { return bf16x8{a[0], a[1], a[2], a[3], b[0], b[1], b[2], b[3]}; }

#define CP_STRIDE 2192

__global__ __launch_bounds__(256) void mid_kernel(const u16* __restrict__ h, const u16* __restrict__ w1,
                                                  const u16* __restrict__ b1, u16* __restrict__ G,
                                                  int dummy) {
  __shared__ __attribute__((aligned(16))) char smem[NS * STAGE_BYTES];
  const int tid = threadIdx.x, wave = tid >> 6, lane = tid & 63;
  {
    const int tile = (blockIdx.x & 7) * 28 + (blockIdx.x >> 3);
    const int bm = tile & 7, bn = tile >> 3;
    const int m0 = bm * 64, n0 = bn * 64;
    f32x4 acc[4][4] = {};
    const u16* srcA[4]; const u16* srcB[4];
#pragma unroll
    for (int i = 0; i < 4; ++i) {
      const unsigned row = wave * 32 + i * 8 + (lane >> 3);
      const unsigned sc = kslow_src_chunk(row, lane & 7);
      srcA[i] = h + (size_t)row * 512 + m0 + sc * 8;
      srcB[i] = w1 + (size_t)row * 1792 + n0 + sc * 8;
    }
    auto issue = [&](int t) {
      char* st = smem + (t % NS) * STAGE_BYTES;
#pragma unroll
      for (int i = 0; i < 4; ++i) {
        GLDS(srcA[i] + (size_t)t * 128 * 512, st + (wave * 32 + i * 8) * 128);
        GLDS(srcB[i] + (size_t)t * 128 * 1792, st + 16384 + (wave * 32 + i * 8) * 128);
      }
    };
    const int NT = 16;
#pragma unroll
    for (int s = 0; s < NS - 1; ++s) issue(s);
    const unsigned g = lane >> 4, q = (lane & 15) >> 2, p = lane & 3;
    const unsigned r0 = wave * 32 + 8 * g + q;
    struct F8 { bf16x4 v[8]; };
    F8 fa[2], fb[2];
    auto load_half = [&](F8& A, unsigned base) {
      tr_read8(base + swz_off(r0, 4 * p), base + swz_off(r0 + 4, 4 * p),
               base + swz_off(r0, 16 + 4 * p), base + swz_off(r0 + 4, 16 + 4 * p), A.v[0], A.v[1], A.v[2], A.v[3]);
      tr_read8(base + swz_off(r0, 32 + 4 * p), base + swz_off(r0 + 4, 32 + 4 * p),
               base + swz_off(r0, 48 + 4 * p), base + swz_off(r0 + 4, 48 + 4 * p), A.v[4], A.v[5], A.v[6], A.v[7]);
    };
    auto compute = [&](F8& A, F8& B) {
#pragma unroll
      for (int i = 0; i < 4; ++i) {
        bf16x8 a = cat4(A.v[2 * i], A.v[2 * i + 1]);
#pragma unroll
        for (int j = 0; j < 4; ++j)
          acc[i][j] = __builtin_amdgcn_mfma_f32_16x16x32_bf16(a, cat4(B.v[2 * j], B.v[2 * j + 1]), acc[i][j], 0, 0, 0);
      }
    };
    auto step = [&](int t, F8& Ac, F8& Bc, F8& An, F8& Bn) {
      if (t + NS - 1 <= NT) WAIT_VM(8); else WAIT_VM(0);
      __builtin_amdgcn_s_barrier();
      if (t + NS - 1 < NT) issue(t + NS - 1);
      if (t + 1 < NT) {
        const unsigned base = (unsigned)(size_t)(smem + ((t + 1) % NS) * STAGE_BYTES);
        load_half(An, base);
        asm volatile("s_waitcnt lgkmcnt(8)" ::: "memory");
        load_half(Bn, base + 16384);
      } else {
        asm volatile("s_waitcnt lgkmcnt(0)" ::: "memory");
      }
      __builtin_amdgcn_sched_barrier(0);
      compute(Ac, Bc);
      __builtin_amdgcn_sched_barrier(0);
    };
    WAIT_VM(16);
    __builtin_amdgcn_s_barrier();
    load_half(fa[0], (unsigned)(size_t)smem);
    load_half(fb[0], (unsigned)(size_t)smem + 16384);
    for (int t = 0; t < NT; t += 2) {
      step(t, fa[0], fb[0], fa[1], fb[1]);
      step(t + 1, fa[1], fb[1], fa[0], fb[0]);
    }
    __syncthreads();
    float* red = (float*)smem;
#pragma unroll
    for (int i = 0; i < 4; ++i)
#pragma unroll
      for (int j = 0; j < 4; ++j)
#pragma unroll
        for (int r = 0; r < 4; ++r)
          red[wave * 4352 + (16 * i + (lane >> 4) * 4 + r) * 68 + 16 * j + (lane & 15)] = acc[i][j][r];
    __syncthreads();
    {
      const int row = tid >> 2, c0 = (tid & 3) * 16;
      bf16x8 o[2];
#pragma unroll
      for (int c = 0; c < 16; ++c) {
        const int o_ = row * 68 + c0 + c;
        float v = (red[o_] + red[4352 + o_]) + (red[2 * 4352 + o_] + red[3 * 4352 + o_]);
        float u = rbf(rbf(v) + bf2f(b1[n0 + c0 + c]));
        o[c >> 3][c & 7] = (short)f2bf(gelu_tanh(u));
      }
      bf16x8* dst = (bf16x8*)(G + (size_t)(m0 + row) * 1792 + n0 + c0);
      dst[0] = o[0]; dst[1] = o[1];
    }
  }
}

__global__ __launch_bounds__(256) void conv_kernel(const u16* __restrict__ xnT, const float* __restrict__ Kdg,
                                                   u16* __restrict__ yT) {
  __shared__ __attribute__((aligned(16))) char smem[35072 + 15360 + 2 * 2112 * 4];
  const int tid = threadIdx.x, wave = tid >> 6, lane = tid & 63;
  const int d = blockIdx.x;
  u16* cp = (u16*)smem;
  u16* Khi = (u16*)(smem + 35072);
  u16* Klo = (u16*)(smem + 35072 + 7680);
  float* red = (float*)(smem + 35072 + 15360);
  float* Kd = red;
  {
    typedef __attribute__((ext_vector_type(4))) unsigned u32x4;
    const u16* up = xnT + (size_t)d * 2048;
    u32x4 w0 = *(const u32x4*)(up + tid * 8);
    u32x4 w1v = {};
    if (tid < 255) w1v = *(const u32x4*)(up + tid * 8 + 8);
    for (int i = tid; i < 641; i += 256) Kd[i] = Kdg[d * 648 + i];
    unsigned dw[8] = {w0[0], w0[1], w0[2], w0[3], w1v[0], w1v[1], w1v[2], w1v[3]};
#pragma unroll
    for (int k = 0; k < 8; ++k) {
      u32x4 o;
#pragma unroll
      for (int j = 0; j < 4; ++j)
        o[j] = (k & 1) ? __builtin_amdgcn_alignbyte(dw[j + (k + 1) / 2], dw[j + (k - 1) / 2], 2) : dw[j + k / 2];
      *(u32x4*)(cp + k * CP_STRIDE + 32 + tid * 8) = o;
    }
    if (tid < 32 * 8) {
      const int k = tid >> 5, xx = (tid & 31) - 32;
      const int ui = xx + k;
      cp[k * CP_STRIDE + 32 + xx] = (ui >= 0) ? up[ui] : (u16)0;
    }
    __syncthreads();
    bf16x8 kh, kl;
#pragma unroll
    for (int e = 0; e < 8; ++e) {
      const int l = (tid >> 2) * 32 + 31 - ((tid & 3) * 8 + e);
      float kv = Kd[idx_of_l(l)];
      u16 hv = f2bf(kv);
      kh[e] = (short)hv;
      kl[e] = (short)f2bf(kv - bf2f(hv));
    }
    const int c = (tid >> 2) + 32, w8 = (tid & 3) * 8;
    *(bf16x8*)(Khi + c * 40 + w8) = kh;
    *(bf16x8*)(Klo + c * 40 + w8) = kl;
    if (tid < 128) {
      const int c2 = tid >> 2;
      bf16x8 z = {};
      *(bf16x8*)(Khi + c2 * 40 + w8) = z;
      *(bf16x8*)(Klo + c2 * 40 + w8) = z;
    }
  }
  __syncthreads();
  f32x16 acc0 = {}, acc1 = {};
  {
    const int r = lane & 31, hh = lane >> 5;
    const u16* abase = cp + ((r + 1) & 7) * CP_STRIDE + 8 * ((r + 1) >> 3) + 8 * hh;
    const int i = lane & 31;
    for (int m = wave; m < 64; m += 4) {
      bf16x8 a0 = *(const bf16x8*)(abase + 32 * m);
      bf16x8 a1 = *(const bf16x8*)(abase + 32 * m + 16);
      {
        const int ch = (32 + i - m + 32) * 40 + 8 * hh;
        bf16x8 bh0 = *(const bf16x8*)(Khi + ch), bh1 = *(const bf16x8*)(Khi + ch + 16);
        bf16x8 bl0 = *(const bf16x8*)(Klo + ch), bl1 = *(const bf16x8*)(Klo + ch + 16);
        acc1 = __builtin_amdgcn_mfma_f32_32x32x16_bf16(a0, bh0, acc1, 0, 0, 0);
        acc1 = __builtin_amdgcn_mfma_f32_32x32x16_bf16(a1, bh1, acc1, 0, 0, 0);
        acc1 = __builtin_amdgcn_mfma_f32_32x32x16_bf16(a0, bl0, acc1, 0, 0, 0);
        acc1 = __builtin_amdgcn_mfma_f32_32x32x16_bf16(a1, bl1, acc1, 0, 0, 0);
      }
      if (m < 32) {
        const int ch = (i - m + 32) * 40 + 8 * hh;
        bf16x8 bh0 = *(const bf16x8*)(Khi + ch), bh1 = *(const bf16x8*)(Khi + ch + 16);
        bf16x8 bl0 = *(const bf16x8*)(Klo + ch), bl1 = *(const bf16x8*)(Klo + ch + 16);
        acc0 = __builtin_amdgcn_mfma_f32_32x32x16_bf16(a0, bh0, acc0, 0, 0, 0);
        acc0 = __builtin_amdgcn_mfma_f32_32x32x16_bf16(a1, bh1, acc0, 0, 0, 0);
        acc0 = __builtin_amdgcn_mfma_f32_32x32x16_bf16(a0, bl0, acc0, 0, 0, 0);
        acc0 = __builtin_amdgcn_mfma_f32_32x32x16_bf16(a1, bl1, acc0, 0, 0, 0);
      }
    }
  }
  {
    const int i = lane & 31, hh = lane >> 5;
    if (wave < 2) {
#pragma unroll
      for (int rg = 0; rg < 16; ++rg) {
        const int row = (rg & 3) + 8 * (rg >> 2) + 4 * hh;
        red[wave * 2112 + i * 33 + row] = acc0[rg];
        red[wave * 2112 + (32 + i) * 33 + row] = acc1[rg];
      }
    }
    __syncthreads();
    if (wave >= 2) {
#pragma unroll
      for (int rg = 0; rg < 16; ++rg) {
        const int row = (rg & 3) + 8 * (rg >> 2) + 4 * hh;
        red[(wave - 2) * 2112 + i * 33 + row] += acc0[rg];
        red[(wave - 2) * 2112 + (32 + i) * 33 + row] += acc1[rg];
      }
    }
  }
  __syncthreads();
  {
    bf16x8 o;
#pragma unroll
    for (int e = 0; e < 8; ++e) {
      const int l = tid * 8 + e;
      const int oo = (l >> 5) * 33 + (l & 31);
      o[e] = (short)f2bf(red[oo] + red[2112 + oo]);
    }
    *(bf16x8*)(yT + (size_t)d * 2048 + tid * 8) = o;
  }
}

__global__ __launch_bounds__(256) void gemm2_kernel(const u16* __restrict__ w2, const u16* __restrict__ G,
                                                    const u16* __restrict__ b2, const u16* __restrict__ x,
                                                    const u16* __restrict__ xn, const u16* __restrict__ yT,
                                                    const u16* __restrict__ dskip, u16* __restrict__ out) {
  __shared__ __attribute__((aligned(16))) char smem[NS * STAGE_BYTES];
  const int tid = threadIdx.x, wave = tid >> 6, lane = tid & 63;
  const int bn = (blockIdx.x >> 3) & 7, bm = (blockIdx.x & 7) * 4 + (blockIdx.x >> 6);
  const int m0 = bm * 64, n0 = bn * 64;
  f32x4 acc[4][4] = {};
  const u16* srcA[4]; const u16* srcB[4];
#pragma unroll
  for (int i = 0; i < 4; ++i) {
    const unsigned row = wave * 32 + i * 8 + (lane >> 3);
    srcA[i] = w2 + (size_t)row * 2048 + m0 + kslow_src_chunk(row, lane & 7) * 8;
    const unsigned nr = row & 63, kh = row >> 6;
    srcB[i] = G + (size_t)(n0 + nr) * 1792 + kh * 64 + (((lane & 7) ^ (nr & 7)) * 8);
  }
  auto issue = [&](int t) {
    char* st = smem + (t % NS) * STAGE_BYTES;
#pragma unroll
    for (int i = 0; i < 4; ++i) {
      GLDS(srcA[i] + (size_t)t * 128 * 2048, st + (wave * 32 + i * 8) * 128);
      GLDS(srcB[i] + t * 128, st + 16384 + (wave * 32 + i * 8) * 128);
    }
  };
  const int NT = 14;
#pragma unroll
  for (int s = 0; s < NS - 1; ++s) issue(s);
  const unsigned g = lane >> 4, q = (lane & 15) >> 2, p = lane & 3;
  const unsigned r0 = wave * 32 + 8 * g + q;
  struct F8 { bf16x4 v[8]; };
  struct FB { bf16x8 v[4]; };
  F8 fa[2]; FB fb[2];
  auto loadA = [&](F8& A, unsigned base) {
    tr_read8(base + swz_off(r0, 4 * p), base + swz_off(r0 + 4, 4 * p),
             base + swz_off(r0, 16 + 4 * p), base + swz_off(r0 + 4, 16 + 4 * p), A.v[0], A.v[1], A.v[2], A.v[3]);
    tr_read8(base + swz_off(r0, 32 + 4 * p), base + swz_off(r0 + 4, 32 + 4 * p),
             base + swz_off(r0, 48 + 4 * p), base + swz_off(r0 + 4, 48 + 4 * p), A.v[4], A.v[5], A.v[6], A.v[7]);
  };
  auto loadB = [&](FB& B, unsigned base) {
    const unsigned bb = base + (wave >> 1) * 8192;
    const unsigned ch = (wave & 1) * 4 + (lane >> 4);
    const unsigned n_0 = (lane & 15), n_1 = n_0 + 16, n_2 = n_0 + 32, n_3 = n_0 + 48;
    b128_read2(bb + n_0 * 128 + ((ch ^ (n_0 & 7)) << 4), bb + n_1 * 128 + ((ch ^ (n_1 & 7)) << 4), B.v[0], B.v[1]);
    b128_read2(bb + n_2 * 128 + ((ch ^ (n_2 & 7)) << 4), bb + n_3 * 128 + ((ch ^ (n_3 & 7)) << 4), B.v[2], B.v[3]);
  };
  auto compute = [&](F8& A, FB& B) {
#pragma unroll
    for (int i = 0; i < 4; ++i) {
      bf16x8 a = cat4(A.v[2 * i], A.v[2 * i + 1]);
#pragma unroll
      for (int j = 0; j < 4; ++j)
        acc[i][j] = __builtin_amdgcn_mfma_f32_16x16x32_bf16(a, B.v[j], acc[i][j], 0, 0, 0);
    }
  };
  auto step = [&](int t, F8& Ac, FB& Bc, F8& An, FB& Bn) {
    if (t + NS - 1 <= NT) WAIT_VM(8); else WAIT_VM(0);
    __builtin_amdgcn_s_barrier();
    if (t + NS - 1 < NT) issue(t + NS - 1);
    if (t + 1 < NT) {
      const unsigned base = (unsigned)(size_t)(smem + ((t + 1) % NS) * STAGE_BYTES);
      loadA(An, base);
      loadB(Bn, base + 16384);
      asm volatile("s_waitcnt lgkmcnt(12)" ::: "memory");
    } else {
      asm volatile("s_waitcnt lgkmcnt(0)" ::: "memory");
    }
    __builtin_amdgcn_sched_barrier(0);
    compute(Ac, Bc);
    __builtin_amdgcn_sched_barrier(0);
  };
  WAIT_VM(16);
  __builtin_amdgcn_s_barrier();
  loadA(fa[0], (unsigned)(size_t)smem);
  loadB(fb[0], (unsigned)(size_t)smem + 16384);
  for (int t = 0; t < NT; t += 2) {
    step(t, fa[0], fb[0], fa[1], fb[1]);
    step(t + 1, fa[1], fb[1], fa[0], fb[0]);
  }
  __syncthreads();
  float* red = (float*)smem;
#pragma unroll
  for (int i = 0; i < 4; ++i)
#pragma unroll
    for (int j = 0; j < 4; ++j)
#pragma unroll
      for (int r = 0; r < 4; ++r)
        red[wave * 4352 + (16 * i + (lane >> 4) * 4 + r) * 68 + 16 * j + (lane & 15)] = acc[i][j][r];
  __syncthreads();
  {
    const int row = tid >> 2, c0 = (tid & 3) * 16;
    const int s = m0 + row;
    const float bb2 = bf2f(b2[s]);
    const bf16x8* xp = (const bf16x8*)(x + (size_t)s * 512 + n0 + c0);
    const bf16x8* xnp = (const bf16x8*)(xn + (size_t)s * 512 + n0 + c0);
    const bf16x8* skp = (const bf16x8*)(dskip + n0 + c0);
    bf16x8 xv[2] = {xp[0], xp[1]}, xnv[2] = {xnp[0], xnp[1]}, skv[2] = {skp[0], skp[1]};
    bf16x8 o[2];
#pragma unroll
    for (int c = 0; c < 16; ++c) {
      const int o_ = row * 68 + c0 + c;
      float v = (red[o_] + red[4352 + o_]) + (red[2 * 4352 + o_] + red[3 * 4352 + o_]);
      float val = rbf(rbf(v) + bb2);
      float a = rbf(bf2f(yT[(size_t)(n0 + c0 + c) * 2048 + s]) + rbf(bf2f((u16)xnv[c >> 3][c & 7]) * bf2f((u16)skv[c >> 3][c & 7])));
      float ov = rbf(rbf(bf2f((u16)xv[c >> 3][c & 7]) + a) + val);
      o[c >> 3][c & 7] = (short)f2bf(ov);
    }
    bf16x8* dst = (bf16x8*)(out + (size_t)s * 512 + n0 + c0);
    dst[0] = o[0]; dst[1] = o[1];
  }
}

extern "C" void kernel_launch(void* const* d_in, const int* in_sizes, int n_in,
                              void* d_out, int out_size, void* d_ws, size_t ws_size,
                              hipStream_t stream) {
  const u16* x = (const u16*)d_in[0];
  const u16* lar = (const u16*)d_in[1];
  const u16* aim = (const u16*)d_in[2];
  const u16* cr = (const u16*)d_in[3];
  const u16* ci = (const u16*)d_in[4];
  const u16* logdt = (const u16*)d_in[5];
  const u16* dskip = (const u16*)d_in[6];
  const u16* w1 = (const u16*)d_in[8];
  const u16* b1 = (const u16*)d_in[9];
  const u16* w2 = (const u16*)d_in[10];
  const u16* b2 = (const u16*)d_in[11];
  u16* out = (u16*)d_out;
  char* ws = (char*)d_ws;
  const size_t SZ = (size_t)2048 * 512 * 2;
  u16* xn = (u16*)(ws);
  u16* h = (u16*)(ws + SZ);
  u16* xnT = (u16*)(ws + 2 * SZ);
  u16* yT = (u16*)(ws + 3 * SZ);
  float* Kdg = (float*)(ws + 4 * SZ);
  u16* G = (u16*)(ws + 6 * SZ);
  prep_kernel<<<1024, 256, 0, stream>>>(x, xn, h, xnT, lar, aim, cr, ci, logdt, Kdg, w1, w2);
  conv_kernel<<<512, 256, 0, stream>>>(xnT, Kdg, yT);
  mid_kernel<<<224, 256, 0, stream>>>(h, w1, b1, G, 0);
  gemm2_kernel<<<256, 256, 0, stream>>>(w2, G, b2, x, xn, yT, dskip, out);
}
```

```cpp
#include <hip/hip_runtime.h>
#include <hip/hip_bf16.h>
#include <stdint.h>

typedef __attribute__((ext_vector_type(8))) short bf16x8;
typedef __attribute__((ext_vector_type(4))) short bf16x4;
typedef __attribute__((ext_vector_type(4))) float f32x4;
typedef __attribute__((ext_vector_type(16))) float f32x16;
typedef unsigned short u16;

__device__ __forceinline__ float bf2f(u16 v) { return __uint_as_float(((unsigned)v) << 16); }
__device__ __forceinline__ u16 f2bf(float f) {
  unsigned u = __float_as_uint(f);
  u += 0x7fffu + ((u >> 16) & 1u);
  return (u16)(u >> 16);
}
__device__ __forceinline__ float rbf(float f) { return bf2f(f2bf(f)); }

template <int CTRL>
__device__ __forceinline__ float dpp_add(float v) {
  return v + __int_as_float(__builtin_amdgcn_update_dpp(0, __float_as_int(v), CTRL, 0xf, 0xf, true));
}
__device__ __forceinline__ float wave_sum(float v) {
  v = dpp_add<0xB1>(v);
  v = dpp_add<0x4E>(v);
  v = dpp_add<0x141>(v);
  v = dpp_add<0x140>(v);
  const int iv = __float_as_int(v);
  const float a = __int_as_float(__builtin_amdgcn_readlane(iv, 0)), b = __int_as_float(__builtin_amdgcn_readlane(iv, 16));
  const float c = __int_as_float(__builtin_amdgcn_readlane(iv, 32)), d = __int_as_float(__builtin_amdgcn_readlane(iv, 48));
  return (a + b) + (c + d);
}

__device__ __forceinline__ void ln8x2(const float (*x)[8], float (*y)[8]) {
  float s[2], q[2], mean[2];
#pragma unroll
  for (int r = 0; r < 2; ++r) {
    s[r] = ((x[r][0] + x[r][1]) + (x[r][2] + x[r][3])) + ((x[r][4] + x[r][5]) + (x[r][6] + x[r][7]));
  }
#pragma unroll
  for (int r = 0; r < 2; ++r) mean[r] = wave_sum(s[r]) * (1.0f / 512.0f);
#pragma unroll
  for (int r = 0; r < 2; ++r) {
    q[r] = 0.f;
#pragma unroll
    for (int i = 0; i < 8; ++i) { float d = x[r][i] - mean[r]; q[r] += d * d; }
  }
#pragma unroll
  for (int r = 0; r < 2; ++r) {
    float var = wave_sum(q[r]) * (1.0f / 512.0f);
    float m_b = rbf(mean[r]);
    float v_b = rbf(var);
    float den = rbf(sqrtf(rbf(v_b + 1e-5f)));
#pragma unroll
    for (int i = 0; i < 8; ++i) y[r][i] = rbf(rbf(x[r][i] - m_b) / den);
  }
}

__device__ __forceinline__ int idx_of_l(int l) {
  int p = (int)rbf((float)l);
  if (p < 256) return p;
  if (p < 512) return 256 + ((p - 256) >> 1);
  if (p < 1024) return 384 + ((p - 512) >> 2);
  return 512 + ((p - 1024) >> 3);
}
__device__ __forceinline__ float2 cexp_acc(float re, float im) {
  float e = expf(re), s, c;
  sincosf(im, &s, &c);
  return make_float2(e * c, e * s);
}
__device__ __forceinline__ float2 cmul(float2 a, float2 b) {
  return make_float2(a.x * b.x - a.y * b.y, a.x * b.y + a.y * b.x);
}

__global__ __launch_bounds__(256) void prep_kernel(const u16* __restrict__ x, u16* __restrict__ xn,
                                                   u16* __restrict__ h, u16* __restrict__ xnT,
                                                   const u16* __restrict__ lar, const u16* __restrict__ aim,
                                                   const u16* __restrict__ cr, const u16* __restrict__ ci,
                                                   const u16* __restrict__ logdt, float* __restrict__ Kdg,
                                                   const u16* __restrict__ w1, const u16* __restrict__ w2) {
  __shared__ __attribute__((aligned(16))) char smem[4 * 8704];
  const int tid = threadIdx.x, wave = tid >> 6, lane = tid & 63;
  const unsigned bid = blockIdx.x;
  if (bid >= 768) {
    typedef __attribute__((ext_vector_type(4))) unsigned u32x4;
    const int j = bid - 768, xc = j & 7, jj = j >> 3;
    u32x4 a[7], b[7];
#pragma unroll
    for (int i = 0; i < 7; ++i) {
      const int c = i * 256 + tid;
      const int r1 = c / 28, c1 = c - r1 * 28;
      a[i] = *(const u32x4*)(w1 + (size_t)(jj * 64 + r1) * 1792 + xc * 224 + c1 * 8);
    }
#pragma unroll
    for (int i = 0; i < 7; ++i) {
      const int c = i * 256 + tid;
      b[i] = *(const u32x4*)(w2 + (size_t)(jj * 56 + (c >> 5)) * 2048 + xc * 256 + (c & 31) * 8);
    }
#pragma unroll
    for (int i = 0; i < 7; ++i) asm volatile("" :: "v"(a[i]), "v"(b[i]));
    return;
  }
  if (bid < 256) {
    u16(*tr)[512 + 8] = (u16(*)[512 + 8])smem;
    const int s0 = ((bid & 7) * 32 + (bid >> 3)) * 8;
    bf16x8 v[2];
#pragma unroll
    for (int rr = 0; rr < 2; ++rr) v[rr] = *(const bf16x8*)(x + (size_t)(s0 + wave * 2 + rr) * 512 + lane * 8);
    float xf[2][8], a[2][8], b[2][8];
#pragma unroll
    for (int rr = 0; rr < 2; ++rr)
#pragma unroll
      for (int i = 0; i < 8; ++i) xf[rr][i] = bf2f((u16)v[rr][i]);
    ln8x2(xf, a);
    ln8x2(a, b);
#pragma unroll
    for (int rr = 0; rr < 2; ++rr) {
      const int r = wave * 2 + rr;
      const int s = s0 + r;
      bf16x8 oa, ob;
#pragma unroll
      for (int i = 0; i < 8; ++i) { oa[i] = (short)f2bf(a[rr][i]); ob[i] = (short)f2bf(b[rr][i]); }
      *(bf16x8*)(&tr[r][lane * 8]) = oa;
      *(bf16x8*)(xn + (size_t)s * 512 + lane * 8) = oa;
      *(bf16x8*)(h + (size_t)s * 512 + lane * 8) = ob;
    }
    __syncthreads();
#pragma unroll
    for (int it = 0; it < 2; ++it) {
      const int d = tid + it * 256;
      bf16x8 o;
#pragma unroll
      for (int i = 0; i < 8; ++i) o[i] = (short)tr[i][d];
      *(bf16x8*)(xnT + (size_t)d * 2048 + s0) = o;
    }
    return;
  }
  const int d = bid - 256, R = wave;
  float* tab = (float*)(smem + wave * 8704);
  const int half = lane >> 5, m = lane & 31;
  u16 pl[4], pi[4], pr[4], pc[4];
#pragma unroll
  for (int c = 0; c < 4; ++c) {
    const int n = d * 128 + c * 32 + m;
    pl[c] = lar[n]; pi[c] = aim[n]; pr[c] = cr[n]; pc[c] = ci[n];
  }
  const float dt = rbf(expf(bf2f(logdt[d])));
  const int numrows = half ? ((R == 0) ? 16 : (R == 3 ? 9 : 8)) : 16;
  const float step = (float)((half ? 16 : 1) << R);
  const float base = (half && R) ? (float)(128 << R) : 0.f;
  const float sgn = half ? -1.f : 1.f;
  float* wp = tab + half * (16 * 68) + 2 * m;
  const float* rpA = tab + 16 * 68 + (lane & 15) * 68 + (lane >> 4) * 4;
  const float* rpB = tab + (lane & 15) * 68 + (lane >> 4) * 4;
  f32x4 acc = {};
#pragma unroll
  for (int c = 0; c < 4; ++c) {
    const float ar = -rbf(expf(bf2f(pl[c]))) * dt;
    const float ai = bf2f(pi[c]) * dt;
    const float2 Cc = half ? make_float2(bf2f(pr[c]) * dt, bf2f(pc[c]) * dt) : make_float2(1.f, 0.f);
    const float2 e = cexp_acc(ar * step, ai * step);
    float2 p = cmul(Cc, cexp_acc(ar * base, ai * base));
#pragma unroll
    for (int r = 0; r < 16; ++r) {
      *(float2*)(wp + r * 68) = (r < numrows) ? make_float2(p.x, sgn * p.y) : make_float2(0.f, 0.f);
      p = cmul(p, e);
    }
    asm volatile("" ::: "memory");
#pragma unroll
    for (int g = 0; g < 4; ++g) {
      const f32x4 av = *(const f32x4*)(rpA + g * 16);
      const f32x4 bv = *(const f32x4*)(rpB + g * 16);
#pragma unroll
      for (int e2 = 0; e2 < 4; ++e2) acc = __builtin_amdgcn_mfma_f32_16x16x4f32(av[e2], bv[e2], acc, 0, 0, 0);
    }
    asm volatile("" ::: "memory");
  }
  const int nout = (R == 0) ? 256 : (R == 3 ? 129 : 128);
  const int off = (R == 0) ? 0 : 128 + 128 * R;
#pragma unroll
  for (int r = 0; r < 4; ++r) {
    const int idx = ((lane >> 4) * 4 + r) * 16 + (lane & 15);
    if (idx < nout) Kdg[d * 648 + off + idx] = 2.0f * acc[r];
  }
}

__device__ __forceinline__ unsigned kslow_src_chunk(unsigned row, unsigned c16) {
  unsigned f = ((row >> 1) & 1) | (((row >> 3) & 1) << 1);
  return ((((c16 >> 1) ^ f)) << 1) | (c16 & 1);
}
__device__ __forceinline__ unsigned swz_off(unsigned row, unsigned col) {
  unsigned c32 = col >> 4;
  unsigned f = ((row >> 1) & 1) | (((row >> 3) & 1) << 1);
  return row * 128 + ((c32 ^ f) << 5) + ((col & 15) << 1);
}
__device__ __forceinline__ float gelu_tanh(float x) {
  float k = 0.7978845608028654f * (x + 0.044715f * x * x * x);
  return x / (1.0f + expf(-2.0f * k));
}
#define GLDS(src, dst) __builtin_amdgcn_global_load_lds((const unsigned*)(src), (unsigned*)(dst), 16, 0, 0)
#define NS 4
#define STAGE_BYTES 32768
#define WAIT_VM(n) asm volatile("s_waitcnt vmcnt(" #n ")" ::: "memory")

__device__ __forceinline__ void tr_read8(unsigned a0, unsigned a1, unsigned a2, unsigned a3,
                                         bf16x4& r0, bf16x4& r1, bf16x4& r2, bf16x4& r3) {
  asm volatile("ds_read_b64_tr_b16 %0, %4\n\tds_read_b64_tr_b16 %1, %5\n\tds_read_b64_tr_b16 %2, %6\n\tds_read_b64_tr_b16 %3, %7"
               : "=&v"(r0), "=&v"(r1), "=&v"(r2), "=&v"(r3) : "v"(a0), "v"(a1), "v"(a2), "v"(a3) : "memory");
}

__device__ __forceinline__ void b128_read2(unsigned a0, unsigned a1, bf16x8& r0, bf16x8& r1) {
  asm volatile("ds_read_b128 %0, %2\n\tds_read_b128 %1, %3" : "=&v"(r0), "=&v"(r1) : "v"(a0), "v"(a1) : "memory");
}
struct FragA { bf16x4 v[2][4]; };
__device__ __forceinline__ bf16x8 cat4(bf16x4 a, bf16x4 b) { return bf16x8{a[0], a[1], a[2], a[3], b[0], b[1], b[2], b[3]}; }

#define CP_STRIDE 2192

__device__ __forceinline__ void team_barrier(unsigned* cnt, unsigned target, int lane) {
  asm volatile("s_waitcnt lgkmcnt(0)" ::: "memory");
  if (lane == 0) __hip_atomic_fetch_add(cnt, 1u, __ATOMIC_RELAXED, __HIP_MEMORY_SCOPE_WORKGROUP);
  while (true) {
    unsigned v = __hip_atomic_load(cnt, __ATOMIC_RELAXED, __HIP_MEMORY_SCOPE_WORKGROUP);
    if (__builtin_amdgcn_readfirstlane(v) >= target) break;
    __builtin_amdgcn_s_sleep(1);
  }
  asm volatile("" ::: "memory");
}

#define NS1 3
#define CONV_OFF (NS1 * STAGE_BYTES)
__global__ __launch_bounds__(512) void mid_kernel(const u16* __restrict__ h, const u16* __restrict__ w1,
                                                  const u16* __restrict__ b1, u16* __restrict__ G,
                                                  const u16* __restrict__ xnT, const float* __restrict__ Kdg,
                                                  u16* __restrict__ yT) {
  __shared__ __attribute__((aligned(16))) char smem[CONV_OFF + 35072 + 15360 + 2592 + 32];
  const int lane = threadIdx.x & 63;
  unsigned* cnts = (unsigned*)(smem + CONV_OFF + 35072 + 15360 + 2592);
  if (threadIdx.x < 8) cnts[threadIdx.x] = 0u;
  __syncthreads();
  if (threadIdx.x < 256) {
    if (blockIdx.x >= 224) return;
    const int tid = threadIdx.x, wave = tid >> 6;
    const int tile = (blockIdx.x & 7) * 28 + (blockIdx.x >> 3);
    const int bm = tile & 7, bn = tile >> 3;
    const int m0 = bm * 64, n0 = bn * 64;
    f32x4 acc[4][4] = {};
    const u16* srcA[4]; const u16* srcB[4];
#pragma unroll
    for (int i = 0; i < 4; ++i) {
      const unsigned row = wave * 32 + i * 8 + (lane >> 3);
      const unsigned sc = kslow_src_chunk(row, lane & 7);
      srcA[i] = h + (size_t)row * 512 + m0 + sc * 8;
      srcB[i] = w1 + (size_t)row * 1792 + n0 + sc * 8;
    }
    auto issue = [&](int t) {
      char* st = smem + (t % NS1) * STAGE_BYTES;
#pragma unroll
      for (int i = 0; i < 4; ++i) {
        GLDS(srcA[i] + (size_t)t * 128 * 512, st + (wave * 32 + i * 8) * 128);
        GLDS(srcB[i] + (size_t)t * 128 * 1792, st + 16384 + (wave * 32 + i * 8) * 128);
      }
    };
    const int NT = 16;
    const bf16x8* b1p = (const bf16x8*)(b1 + n0 + (tid & 3) * 16);
    const bf16x8 b1v[2] = {b1p[0], b1p[1]};
    asm volatile("" ::: "memory");
#pragma unroll
    for (int s = 0; s < NS1; ++s) issue(s);
    const unsigned g = lane >> 4, q = (lane & 15) >> 2, p = lane & 3;
    const unsigned r0 = wave * 32 + 8 * g + q;
    struct F8 { bf16x4 v[8]; };
    F8 fa[2], fb[2];
    auto load_half = [&](F8& A, unsigned base, bool lim) {
      tr_read8(base + swz_off(r0, 4 * p), base + swz_off(r0 + 4, 4 * p),
               base + swz_off(r0, 16 + 4 * p), base + swz_off(r0 + 4, 16 + 4 * p), A.v[0], A.v[1], A.v[2], A.v[3]);
      if (lim) asm volatile("s_waitcnt lgkmcnt(8)" ::: "memory");
      tr_read8(base + swz_off(r0, 32 + 4 * p), base + swz_off(r0 + 4, 32 + 4 * p),
               base + swz_off(r0, 48 + 4 * p), base + swz_off(r0 + 4, 48 + 4 * p), A.v[4], A.v[5], A.v[6], A.v[7]);
    };
    auto compute = [&](F8& A, F8& B) {
#pragma unroll
      for (int i = 0; i < 4; ++i) {
        bf16x8 a = cat4(A.v[2 * i], A.v[2 * i + 1]);
#pragma unroll
        for (int j = 0; j < 4; ++j)
          acc[i][j] = __builtin_amdgcn_mfma_f32_16x16x32_bf16(a, cat4(B.v[2 * j], B.v[2 * j + 1]), acc[i][j], 0, 0, 0);
      }
    };
    auto step = [&](int t, F8& Ac, F8& Bc, F8& An, F8& Bn) {
      if (t + 1 < NT) {
        if (t + 2 < NT) WAIT_VM(8); else WAIT_VM(0);
        asm volatile("s_waitcnt lgkmcnt(0)" ::: "memory");
        if (t + 3 < NT) issue(t + 3);
        const unsigned base = (unsigned)(size_t)(smem + ((t + 1) % NS1) * STAGE_BYTES);
        load_half(An, base, false);
        load_half(Bn, base + 16384, true);
      } else {
        asm volatile("s_waitcnt lgkmcnt(0)" ::: "memory");
      }
      __builtin_amdgcn_sched_barrier(0);
      compute(Ac, Bc);
      __builtin_amdgcn_sched_barrier(0);
    };
    WAIT_VM(16);
    load_half(fa[0], (unsigned)(size_t)smem, false);
    load_half(fb[0], (unsigned)(size_t)smem + 16384, true);
    for (int t = 0; t < NT; t += 2) {
      step(t, fa[0], fb[0], fa[1], fb[1]);
      step(t + 1, fa[1], fb[1], fa[0], fb[0]);
    }
    team_barrier(cnts, 4u, lane);
    float* red = (float*)smem;
#pragma unroll
    for (int i = 0; i < 4; ++i)
#pragma unroll
      for (int j = 0; j < 4; ++j)
#pragma unroll
        for (int r = 0; r < 4; ++r)
          red[wave * 4352 + (16 * i + (lane >> 4) * 4 + r) * 68 + 16 * j + (lane & 15)] = acc[i][j][r];
    team_barrier(cnts, 8u, lane);
    {
      const int row = tid >> 2, c0 = (tid & 3) * 16;
      bf16x8 o[2];
#pragma unroll
      for (int c4 = 0; c4 < 4; ++c4) {
        const f32x4* rp = (const f32x4*)(red + row * 68 + c0 + c4 * 4);
        const f32x4 v4 = (rp[0] + rp[1088]) + (rp[2 * 1088] + rp[3 * 1088]);
#pragma unroll
        for (int e = 0; e < 4; ++e) {
          const int c = c4 * 4 + e;
          float u = rbf(rbf(v4[e]) + bf2f((u16)b1v[c >> 3][c & 7]));
          o[c >> 3][c & 7] = (short)f2bf(gelu_tanh(u));
        }
      }
      bf16x8* dst = (bf16x8*)(G + (size_t)(m0 + row) * 1792 + n0 + c0);
      dst[0] = o[0]; dst[1] = o[1];
    }
    return;
  }
  typedef __attribute__((ext_vector_type(4))) unsigned u32x4;
  const int tid = threadIdx.x - 256, wave = tid >> 6;
  unsigned* cnt = cnts + 4;
  u16* cp = (u16*)(smem + CONV_OFF);
  u16* Khi = (u16*)(smem + CONV_OFF + 35072);
  u16* Klo = (u16*)(smem + CONV_OFF + 35072 + 7680);
  float* red = (float*)cp;
  unsigned phase = 0;
  u32x4 w0[2], w1v[2];
  float kv[2][8];
  u16 padv[2];
  const int pk = tid >> 5, pxx = (tid & 31) - 32;
#pragma unroll
  for (int ch = 0; ch < 2; ++ch) {
    const int d = blockIdx.x * 2 + ch;
    const u16* up = xnT + (size_t)d * 2048;
    w0[ch] = *(const u32x4*)(up + tid * 8);
    w1v[ch] = u32x4{0u, 0u, 0u, 0u};
    if (tid < 255) w1v[ch] = *(const u32x4*)(up + tid * 8 + 8);
    padv[ch] = (pxx + pk >= 0) ? up[pxx + pk] : (u16)0;
#pragma unroll
    for (int e = 0; e < 8; ++e) {
      const int l = (tid >> 2) * 32 + 31 - ((tid & 3) * 8 + e);
      kv[ch][e] = Kdg[d * 648 + idx_of_l(l)];
    }
  }
  if (tid < 128) {
    const int c2 = tid >> 2, w8 = (tid & 3) * 8;
    bf16x8 z = {};
    *(bf16x8*)(Khi + c2 * 40 + w8) = z;
    *(bf16x8*)(Klo + c2 * 40 + w8) = z;
  }
#pragma unroll
  for (int ch = 0; ch < 2; ++ch) {
    const int d = blockIdx.x * 2 + ch;
    {
      unsigned dw[8] = {w0[ch][0], w0[ch][1], w0[ch][2], w0[ch][3], w1v[ch][0], w1v[ch][1], w1v[ch][2], w1v[ch][3]};
#pragma unroll
      for (int k = 0; k < 8; ++k) {
        u32x4 o;
#pragma unroll
        for (int j = 0; j < 4; ++j)
          o[j] = (k & 1) ? __builtin_amdgcn_alignbyte(dw[j + (k + 1) / 2], dw[j + (k - 1) / 2], 2) : dw[j + k / 2];
        *(u32x4*)(cp + k * CP_STRIDE + 32 + tid * 8) = o;
      }
      cp[pk * CP_STRIDE + 32 + pxx] = padv[ch];
      bf16x8 kh, kl;
#pragma unroll
      for (int e = 0; e < 8; ++e) {
        u16 hv = f2bf(kv[ch][e]);
        kh[e] = (short)hv;
        kl[e] = (short)f2bf(kv[ch][e] - bf2f(hv));
      }
      const int c = (tid >> 2) + 32, w8 = (tid & 3) * 8;
      *(bf16x8*)(Khi + c * 40 + w8) = kh;
      *(bf16x8*)(Klo + c * 40 + w8) = kl;
    }
    team_barrier(cnt, phase += 4, lane);
    f32x16 acc0 = {}, acc1 = {};
    {
      const int r = lane & 31, hh = lane >> 5;
      const u16* abase = cp + ((r + 1) & 7) * CP_STRIDE + 8 * ((r + 1) >> 3) + 8 * hh + 32 * wave;
      const u16* kb = Khi + (r - wave + 32) * 40 + 8 * hh;
      struct Fr { bf16x8 bh0, bh1, bl0, bl1, a0, a1, c0, c1; };
      Fr f[2];
      auto ld = [&](Fr& F, int st) {
        const int t4 = (st < 8) ? 4 * st : 4 * (st - 8) - 32;
        const u16* kp = kb - t4 * 40;
        F.bh0 = *(const bf16x8*)(kp); F.bh1 = *(const bf16x8*)(kp + 16);
        F.bl0 = *(const bf16x8*)(kp + 3840); F.bl1 = *(const bf16x8*)(kp + 3840 + 16);
        F.c0 = *(const bf16x8*)(abase + 32 * (t4 + 32)); F.c1 = *(const bf16x8*)(abase + 32 * (t4 + 32) + 16);
        if (st < 8) { F.a0 = *(const bf16x8*)(abase + 32 * t4); F.a1 = *(const bf16x8*)(abase + 32 * t4 + 16); }
      };
      ld(f[0], 0);
#pragma unroll
      for (int st = 0; st < 16; ++st) {
        if (st + 1 < 16) ld(f[(st + 1) & 1], st + 1);
        Fr& F = f[st & 1];
        acc1 = __builtin_amdgcn_mfma_f32_32x32x16_bf16(F.c0, F.bh0, acc1, 0, 0, 0);
        if (st < 8) acc0 = __builtin_amdgcn_mfma_f32_32x32x16_bf16(F.a0, F.bh0, acc0, 0, 0, 0);
        acc1 = __builtin_amdgcn_mfma_f32_32x32x16_bf16(F.c1, F.bh1, acc1, 0, 0, 0);
        if (st < 8) acc0 = __builtin_amdgcn_mfma_f32_32x32x16_bf16(F.a1, F.bh1, acc0, 0, 0, 0);
        acc1 = __builtin_amdgcn_mfma_f32_32x32x16_bf16(F.c0, F.bl0, acc1, 0, 0, 0);
        if (st < 8) acc0 = __builtin_amdgcn_mfma_f32_32x32x16_bf16(F.a0, F.bl0, acc0, 0, 0, 0);
        acc1 = __builtin_amdgcn_mfma_f32_32x32x16_bf16(F.c1, F.bl1, acc1, 0, 0, 0);
        if (st < 8) acc0 = __builtin_amdgcn_mfma_f32_32x32x16_bf16(F.a1, F.bl1, acc0, 0, 0, 0);
      }
    }
    team_barrier(cnt, phase += 4, lane);
    {
      const int i = lane & 31, hh = lane >> 5;
#pragma unroll
      for (int rg = 0; rg < 16; ++rg) {
        const int row = (rg & 3) + 8 * (rg >> 2) + 4 * hh;
        red[wave * 2112 + i * 33 + row] = acc0[rg];
        red[wave * 2112 + (32 + i) * 33 + row] = acc1[rg];
      }
    }
    team_barrier(cnt, phase += 4, lane);
    {
      bf16x8 o;
#pragma unroll
      for (int e = 0; e < 8; ++e) {
        const int l = tid * 8 + e;
        const int oo = (l >> 5) * 33 + (l & 31);
        o[e] = (short)f2bf((red[oo] + red[2112 + oo]) + (red[2 * 2112 + oo] + red[3 * 2112 + oo]));
      }
      *(bf16x8*)(yT + (size_t)d * 2048 + tid * 8) = o;
    }
    if (ch == 0) team_barrier(cnt, phase += 4, lane);
  }
}

__global__ __launch_bounds__(256) void gemm2_kernel(const u16* __restrict__ w2, const u16* __restrict__ G,
                                                    const u16* __restrict__ b2, const u16* __restrict__ x,
                                                    const u16* __restrict__ xn, const u16* __restrict__ yT,
                                                    const u16* __restrict__ dskip, u16* __restrict__ out) {
  __shared__ __attribute__((aligned(16))) char smem[NS * STAGE_BYTES + 64 * 144];
  const int tid = threadIdx.x, wave = tid >> 6, lane = tid & 63;
  const int bn = (blockIdx.x >> 3) & 7, bm = (blockIdx.x & 7) * 4 + (blockIdx.x >> 6);
  const int m0 = bm * 64, n0 = bn * 64;
  f32x4 acc[4][4] = {};
  const u16* srcA[4]; const u16* srcB[4];
#pragma unroll
  for (int i = 0; i < 4; ++i) {
    const unsigned row = wave * 32 + i * 8 + (lane >> 3);
    srcA[i] = w2 + (size_t)row * 2048 + m0 + kslow_src_chunk(row, lane & 7) * 8;
    const unsigned nr = row & 63, kh = row >> 6;
    srcB[i] = G + (size_t)(n0 + nr) * 1792 + kh * 64 + (((lane & 7) ^ (nr & 7)) * 8);
  }
  auto issue = [&](int t) {
    char* st = smem + (t % NS) * STAGE_BYTES;
#pragma unroll
    for (int i = 0; i < 4; ++i) {
      GLDS(srcA[i] + (size_t)t * 128 * 2048, st + (wave * 32 + i * 8) * 128);
      GLDS(srcB[i] + t * 128, st + 16384 + (wave * 32 + i * 8) * 128);
    }
  };
  const int NT = 14;
#pragma unroll
  for (int s = 0; s < NS - 1; ++s) issue(s);
  asm volatile("" ::: "memory");
  u16* ytile = (u16*)(smem + NS * STAGE_BYTES);
  const int erow = tid >> 2, ec0 = (tid & 3) * 16;
  const bf16x8 yv0 = *(const bf16x8*)(yT + (size_t)(n0 + (tid >> 3)) * 2048 + m0 + (tid & 7) * 8);
  const bf16x8 yv1 = *(const bf16x8*)(yT + (size_t)(n0 + 32 + (tid >> 3)) * 2048 + m0 + (tid & 7) * 8);
  const bf16x8* xp = (const bf16x8*)(x + (size_t)(m0 + erow) * 512 + n0 + ec0);
  const bf16x8* xnp = (const bf16x8*)(xn + (size_t)(m0 + erow) * 512 + n0 + ec0);
  const bf16x8* skp = (const bf16x8*)(dskip + n0 + ec0);
  const bf16x8 xv[2] = {xp[0], xp[1]}, xnv[2] = {xnp[0], xnp[1]}, skv[2] = {skp[0], skp[1]};
  const u16 bb2r = b2[m0 + erow];
  asm volatile("" ::: "memory");
  const unsigned g = lane >> 4, q = (lane & 15) >> 2, p = lane & 3;
  const unsigned r0 = wave * 32 + 8 * g + q;
  struct F8 { bf16x4 v[8]; };
  struct FB { bf16x8 v[4]; };
  F8 fa[2]; FB fb[2];
  auto loadA = [&](F8& A, unsigned base) {
    tr_read8(base + swz_off(r0, 4 * p), base + swz_off(r0 + 4, 4 * p),
             base + swz_off(r0, 16 + 4 * p), base + swz_off(r0 + 4, 16 + 4 * p), A.v[0], A.v[1], A.v[2], A.v[3]);
    tr_read8(base + swz_off(r0, 32 + 4 * p), base + swz_off(r0 + 4, 32 + 4 * p),
             base + swz_off(r0, 48 + 4 * p), base + swz_off(r0 + 4, 48 + 4 * p), A.v[4], A.v[5], A.v[6], A.v[7]);
  };
  auto loadB = [&](FB& B, unsigned base) {
    const unsigned bb = base + (wave >> 1) * 8192;
    const unsigned ch = (wave & 1) * 4 + (lane >> 4);
    const unsigned n_0 = (lane & 15), n_1 = n_0 + 16, n_2 = n_0 + 32, n_3 = n_0 + 48;
    b128_read2(bb + n_0 * 128 + ((ch ^ (n_0 & 7)) << 4), bb + n_1 * 128 + ((ch ^ (n_1 & 7)) << 4), B.v[0], B.v[1]);
    b128_read2(bb + n_2 * 128 + ((ch ^ (n_2 & 7)) << 4), bb + n_3 * 128 + ((ch ^ (n_3 & 7)) << 4), B.v[2], B.v[3]);
  };
  auto compute = [&](F8& A, FB& B) {
#pragma unroll
    for (int i = 0; i < 4; ++i) {
      bf16x8 a = cat4(A.v[2 * i], A.v[2 * i + 1]);
#pragma unroll
      for (int j = 0; j < 4; ++j)
        acc[i][j] = __builtin_amdgcn_mfma_f32_16x16x32_bf16(a, B.v[j], acc[i][j], 0, 0, 0);
    }
  };
  auto step = [&](int t, F8& Ac, FB& Bc, F8& An, FB& Bn) {
    if (t < 2) WAIT_VM(17); else if (t + NS - 1 <= NT) WAIT_VM(8); else WAIT_VM(0);
    __builtin_amdgcn_s_barrier();
    if (t + NS - 1 < NT) issue(t + NS - 1);
    if (t + 1 < NT) {
      const unsigned base = (unsigned)(size_t)(smem + ((t + 1) % NS) * STAGE_BYTES);
      asm volatile("s_waitcnt lgkmcnt(0)" ::: "memory");
      loadA(An, base);
      loadB(Bn, base + 16384);
    } else {
      asm volatile("s_waitcnt lgkmcnt(0)" ::: "memory");
    }
    __builtin_amdgcn_sched_barrier(0);
    compute(Ac, Bc);
    __builtin_amdgcn_sched_barrier(0);
  };
  WAIT_VM(25);
  __builtin_amdgcn_s_barrier();
  loadA(fa[0], (unsigned)(size_t)smem);
  loadB(fb[0], (unsigned)(size_t)smem + 16384);
  for (int t = 0; t < NT; t += 2) {
    step(t, fa[0], fb[0], fa[1], fb[1]);
    step(t + 1, fa[1], fb[1], fa[0], fb[0]);
  }
  *(bf16x8*)(ytile + (tid >> 3) * 72 + (tid & 7) * 8) = yv0;
  *(bf16x8*)(ytile + (32 + (tid >> 3)) * 72 + (tid & 7) * 8) = yv1;
  __syncthreads();
  float* red = (float*)smem;
#pragma unroll
  for (int i = 0; i < 4; ++i)
#pragma unroll
    for (int j = 0; j < 4; ++j)
#pragma unroll
      for (int r = 0; r < 4; ++r)
        red[wave * 4352 + (16 * i + (lane >> 4) * 4 + r) * 68 + 16 * j + (lane & 15)] = acc[i][j][r];
  __syncthreads();
  {
    const int row = erow, c0 = ec0;
    const int s = m0 + row;
    const float bb2 = bf2f(bb2r);
    bf16x8 o[2];
#pragma unroll
    for (int c4 = 0; c4 < 4; ++c4) {
      const f32x4* rp = (const f32x4*)(red + row * 68 + c0 + c4 * 4);
      const f32x4 v4 = (rp[0] + rp[1088]) + (rp[2 * 1088] + rp[3 * 1088]);
#pragma unroll
      for (int e = 0; e < 4; ++e) {
        const int c = c4 * 4 + e;
        float val = rbf(rbf(v4[e]) + bb2);
        float a = rbf(bf2f(ytile[(c0 + c) * 72 + row]) + rbf(bf2f((u16)xnv[c >> 3][c & 7]) * bf2f((u16)skv[c >> 3][c & 7])));
        float ov = rbf(rbf(bf2f((u16)xv[c >> 3][c & 7]) + a) + val);
        o[c >> 3][c & 7] = (short)f2bf(ov);
      }
    }
    bf16x8* dst = (bf16x8*)(out + (size_t)s * 512 + n0 + c0);
    dst[0] = o[0]; dst[1] = o[1];
  }
}

extern "C" void kernel_launch(void* const* d_in, const int* in_sizes, int n_in,
                              void* d_out, int out_size, void* d_ws, size_t ws_size,
                              hipStream_t stream) {
  const u16* x = (const u16*)d_in[0];
  const u16* lar = (const u16*)d_in[1];
  const u16* aim = (const u16*)d_in[2];
  const u16* cr = (const u16*)d_in[3];
  const u16* ci = (const u16*)d_in[4];
  const u16* logdt = (const u16*)d_in[5];
  const u16* dskip = (const u16*)d_in[6];
  const u16* w1 = (const u16*)d_in[8];
  const u16* b1 = (const u16*)d_in[9];
  const u16* w2 = (const u16*)d_in[10];
  const u16* b2 = (const u16*)d_in[11];
  u16* out = (u16*)d_out;
  char* ws = (char*)d_ws;
  const size_t SZ = (size_t)2048 * 512 * 2;
  u16* xn = (u16*)(ws);
  u16* h = (u16*)(ws + SZ);
  u16* xnT = (u16*)(ws + 2 * SZ);
  u16* yT = (u16*)(ws + 3 * SZ);
  float* Kdg = (float*)(ws + 4 * SZ);
  u16* G = (u16*)(ws + 6 * SZ);
  prep_kernel<<<1024, 256, 0, stream>>>(x, xn, h, xnT, lar, aim, cr, ci, logdt, Kdg, w1, w2);
  mid_kernel<<<256, 512, 0, stream>>>(h, w1, b1, G, xnT, Kdg, yT);
  gemm2_kernel<<<256, 256, 0, stream>>>(w2, G, b2, x, xn, yT, dskip, out);
}
```

```cpp
#include <hip/hip_runtime.h>
#include <hip/hip_bf16.h>
#include <stdint.h>

typedef __attribute__((ext_vector_type(8))) short bf16x8;
typedef __attribute__((ext_vector_type(4))) short bf16x4;
typedef __attribute__((ext_vector_type(4))) float f32x4;
typedef __attribute__((ext_vector_type(16))) float f32x16;
typedef unsigned short u16;

__device__ __forceinline__ float bf2f(u16 v) { return __uint_as_float(((unsigned)v) << 16); }
__device__ __forceinline__ u16 f2bf(float f) { return __builtin_bit_cast(u16, (__bf16)f); }
__device__ __forceinline__ float rbf(float f) { return bf2f(f2bf(f)); }
typedef __attribute__((ext_vector_type(2))) float f32x2;
__device__ __forceinline__ f32x2 cmul2(f32x2 a, f32x2 b) {
  const f32x2 t = a.xx * b;
  const f32x2 bs = {-b.y, b.x};
  return __builtin_elementwise_fma(a.yy, bs, t);
}

template <int CTRL>
__device__ __forceinline__ float dpp_add(float v) {
  return v + __int_as_float(__builtin_amdgcn_update_dpp(0, __float_as_int(v), CTRL, 0xf, 0xf, true));
}
__device__ __forceinline__ float wave_sum(float v) {
  v = dpp_add<0xB1>(v);
  v = dpp_add<0x4E>(v);
  v = dpp_add<0x141>(v);
  v = dpp_add<0x140>(v);
  const int iv = __float_as_int(v);
  const float a = __int_as_float(__builtin_amdgcn_readlane(iv, 0)), b = __int_as_float(__builtin_amdgcn_readlane(iv, 16));
  const float c = __int_as_float(__builtin_amdgcn_readlane(iv, 32)), d = __int_as_float(__builtin_amdgcn_readlane(iv, 48));
  return (a + b) + (c + d);
}

__device__ __forceinline__ void ln8x2(const float (*x)[8], float (*y)[8]) {
  float s[2], q[2], mean[2];
#pragma unroll
  for (int r = 0; r < 2; ++r) {
    s[r] = ((x[r][0] + x[r][1]) + (x[r][2] + x[r][3])) + ((x[r][4] + x[r][5]) + (x[r][6] + x[r][7]));
  }
#pragma unroll
  for (int r = 0; r < 2; ++r) mean[r] = wave_sum(s[r]) * (1.0f / 512.0f);
#pragma unroll
  for (int r = 0; r < 2; ++r) {
    q[r] = 0.f;
#pragma unroll
    for (int i = 0; i < 8; ++i) { float d = x[r][i] - mean[r]; q[r] += d * d; }
  }
#pragma unroll
  for (int r = 0; r < 2; ++r) {
    float var = wave_sum(q[r]) * (1.0f / 512.0f);
    float m_b = rbf(mean[r]);
    float v_b = rbf(var);
    float den = rbf(sqrtf(rbf(v_b + 1e-5f)));
    const float rden = 1.0f / den;
#pragma unroll
    for (int i = 0; i < 8; ++i) {
      const float nn = rbf(x[r][i] - m_b);
      const float q0 = nn * rden;
      y[r][i] = rbf(fmaf(fmaf(-q0, den, nn), rden, q0));
    }
  }
}

__device__ __forceinline__ int idx_of_l(int l) {
  int p = (int)rbf((float)l);
  if (p < 256) return p;
  if (p < 512) return 256 + ((p - 256) >> 1);
  if (p < 1024) return 384 + ((p - 512) >> 2);
  return 512 + ((p - 1024) >> 3);
}
__device__ __forceinline__ float2 cexp_acc(float re, float im) {
  float e = expf(re), s, c;
  sincosf(im, &s, &c);
  return make_float2(e * c, e * s);
}
__device__ __forceinline__ float2 cmul(float2 a, float2 b) {
  return make_float2(a.x * b.x - a.y * b.y, a.x * b.y + a.y * b.x);
}

__global__ __launch_bounds__(256) void prep_kernel(const u16* __restrict__ x, u16* __restrict__ xn,
                                                   u16* __restrict__ h, u16* __restrict__ xnT,
                                                   const u16* __restrict__ lar, const u16* __restrict__ aim,
                                                   const u16* __restrict__ cr, const u16* __restrict__ ci,
                                                   const u16* __restrict__ logdt, float* __restrict__ Kdg,
                                                   const u16* __restrict__ w1, const u16* __restrict__ w2) {
  __shared__ __attribute__((aligned(16))) char smem[4 * 8704 + 2048];
  const int tid = threadIdx.x, wave = tid >> 6, lane = tid & 63;
  const unsigned bid = blockIdx.x;
  if (bid >= 768) {
    typedef __attribute__((ext_vector_type(4))) unsigned u32x4;
    const int j = bid - 768, xc = j & 7, jj = j >> 3;
    u32x4 a[7], b[7];
#pragma unroll
    for (int i = 0; i < 7; ++i) {
      const int c = i * 256 + tid;
      const int r1 = c / 28, c1 = c - r1 * 28;
      a[i] = *(const u32x4*)(w1 + (size_t)(jj * 64 + r1) * 1792 + xc * 224 + c1 * 8);
    }
#pragma unroll
    for (int i = 0; i < 7; ++i) {
      const int c = i * 256 + tid;
      b[i] = *(const u32x4*)(w2 + (size_t)(jj * 56 + (c >> 5)) * 2048 + xc * 256 + (c & 31) * 8);
    }
#pragma unroll
    for (int i = 0; i < 7; ++i) asm volatile("" :: "v"(a[i]), "v"(b[i]));
    return;
  }
  if (bid < 256) {
    u16(*tr)[512 + 8] = (u16(*)[512 + 8])smem;
    const int s0 = ((bid & 7) * 32 + (bid >> 3)) * 8;
    bf16x8 v[2];
#pragma unroll
    for (int rr = 0; rr < 2; ++rr) v[rr] = *(const bf16x8*)(x + (size_t)(s0 + wave * 2 + rr) * 512 + lane * 8);
    float xf[2][8], a[2][8], b[2][8];
#pragma unroll
    for (int rr = 0; rr < 2; ++rr)
#pragma unroll
      for (int i = 0; i < 8; ++i) xf[rr][i] = bf2f((u16)v[rr][i]);
    ln8x2(xf, a);
    ln8x2(a, b);
#pragma unroll
    for (int rr = 0; rr < 2; ++rr) {
      const int r = wave * 2 + rr;
      const int s = s0 + r;
      bf16x8 oa, ob;
#pragma unroll
      for (int i = 0; i < 8; ++i) { oa[i] = (short)f2bf(a[rr][i]); ob[i] = (short)f2bf(b[rr][i]); }
      *(bf16x8*)(&tr[r][lane * 8]) = oa;
      *(bf16x8*)(xn + (size_t)s * 512 + lane * 8) = oa;
      *(bf16x8*)(h + (size_t)s * 512 + lane * 8) = ob;
    }
    __syncthreads();
#pragma unroll
    for (int it = 0; it < 2; ++it) {
      const int d = tid + it * 256;
      bf16x8 o;
#pragma unroll
      for (int i = 0; i < 8; ++i) o[i] = (short)tr[i][d];
      *(bf16x8*)(xnT + (size_t)d * 2048 + s0) = o;
    }
    return;
  }
  const int d = bid - 256, R = wave;
  float* tab = (float*)(smem + wave * 8704);
  f32x2* Etab = (f32x2*)(smem + 4 * 8704);
  const int half = lane >> 5, m = lane & 31;
  u16 pr[4], pc[4];
#pragma unroll
  for (int c = 0; c < 4; ++c) {
    const int n = d * 128 + c * 32 + m;
    pr[c] = cr[n]; pc[c] = ci[n];
  }
  const float dt = rbf(expf(bf2f(logdt[d])));
  {
    const int n = d * 128 + (tid & 127);
    const float st0 = (tid >> 7) ? 16.f : 1.f;
    const float ar = -rbf(expf(bf2f(lar[n]))) * dt;
    const float ai = bf2f(aim[n]) * dt;
    const float2 e = cexp_acc(ar * st0, ai * st0);
    Etab[tid] = f32x2{e.x, e.y};
  }
  __syncthreads();
  const int numrows = half ? ((R == 0) ? 16 : (R == 3 ? 9 : 8)) : 16;
  const bool usebase = half && R;
  const f32x2 sgn = {1.f, half ? -1.f : 1.f};
  float* wp = tab + half * (16 * 68) + 2 * m;
  const float* rpA = tab + 16 * 68 + (lane & 15) * 68 + (lane >> 4) * 4;
  const float* rpB = tab + (lane & 15) * 68 + (lane >> 4) * 4;
  f32x2 e1[4];
#pragma unroll
  for (int c = 0; c < 4; ++c) e1[c] = Etab[half * 128 + c * 32 + m];
  for (int k = 0; k < R; ++k) {
#pragma unroll
    for (int c = 0; c < 4; ++c) e1[c] = cmul2(e1[c], e1[c]);
  }
  f32x4 acc4[4] = {};
#pragma unroll
  for (int c = 0; c < 4; ++c) {
    const f32x2 Cc = half ? f32x2{bf2f(pr[c]) * dt, bf2f(pc[c]) * dt} : f32x2{1.f, 0.f};
    const f32x2 e2 = cmul2(e1[c], e1[c]), e4 = cmul2(e2, e2), e8 = cmul2(e4, e4);
    f32x2 p[16];
    p[0] = usebase ? cmul2(Cc, e8) : Cc;
    p[1] = cmul2(p[0], e1[c]);
#pragma unroll
    for (int r = 0; r < 2; ++r) p[2 + r] = cmul2(p[r], e2);
#pragma unroll
    for (int r = 0; r < 4; ++r) p[4 + r] = cmul2(p[r], e4);
#pragma unroll
    for (int r = 0; r < 8; ++r) p[8 + r] = cmul2(p[r], e8);
#pragma unroll
    for (int r = 0; r < 16; ++r)
      *(f32x2*)(wp + r * 68) = (r < 8 || r < numrows) ? p[r] * sgn : f32x2{0.f, 0.f};
    asm volatile("" ::: "memory");
#pragma unroll
    for (int g = 0; g < 4; ++g) {
      const f32x4 av = *(const f32x4*)(rpA + g * 16);
      const f32x4 bv = *(const f32x4*)(rpB + g * 16);
#pragma unroll
      for (int e2_ = 0; e2_ < 4; ++e2_) acc4[e2_] = __builtin_amdgcn_mfma_f32_16x16x4f32(av[e2_], bv[e2_], acc4[e2_], 0, 0, 0);
    }
    asm volatile("" ::: "memory");
  }
  const f32x4 acc = (acc4[0] + acc4[1]) + (acc4[2] + acc4[3]);
  const int nout = (R == 0) ? 256 : (R == 3 ? 129 : 128);
  const int off = (R == 0) ? 0 : 128 + 128 * R;
#pragma unroll
  for (int r = 0; r < 4; ++r) {
    const int idx = ((lane >> 4) * 4 + r) * 16 + (lane & 15);
    if (idx < nout) Kdg[d * 648 + off + idx] = 2.0f * acc[r];
  }
}

__device__ __forceinline__ unsigned kslow_src_chunk(unsigned row, unsigned c16) {
  unsigned f = ((row >> 1) & 1) | (((row >> 3) & 1) << 1);
  return ((((c16 >> 1) ^ f)) << 1) | (c16 & 1);
}
__device__ __forceinline__ unsigned swz_off(unsigned row, unsigned col) {
  unsigned c32 = col >> 4;
  unsigned f = ((row >> 1) & 1) | (((row >> 3) & 1) << 1);
  return row * 128 + ((c32 ^ f) << 5) + ((col & 15) << 1);
}
__device__ __forceinline__ float gelu_tanh(float x) {
  float k = 0.7978845608028654f * (x + 0.044715f * x * x * x);
  return x / (1.0f + expf(-2.0f * k));
}
#define GLDS(src, dst) __builtin_amdgcn_global_load_lds((const unsigned*)(src), (unsigned*)(dst), 16, 0, 0)
#define NS 4
#define STAGE_BYTES 32768
#define WAIT_VM(n) asm volatile("s_waitcnt vmcnt(" #n ")" ::: "memory")

__device__ __forceinline__ void tr_read8(unsigned a0, unsigned a1, unsigned a2, unsigned a3,
                                         bf16x4& r0, bf16x4& r1, bf16x4& r2, bf16x4& r3) {
  asm volatile("ds_read_b64_tr_b16 %0, %4\n\tds_read_b64_tr_b16 %1, %5\n\tds_read_b64_tr_b16 %2, %6\n\tds_read_b64_tr_b16 %3, %7"
               : "=&v"(r0), "=&v"(r1), "=&v"(r2), "=&v"(r3) : "v"(a0), "v"(a1), "v"(a2), "v"(a3) : "memory");
}

__device__ __forceinline__ void b128_read2(unsigned a0, unsigned a1, bf16x8& r0, bf16x8& r1) {
  asm volatile("ds_read_b128 %0, %2\n\tds_read_b128 %1, %3" : "=&v"(r0), "=&v"(r1) : "v"(a0), "v"(a1) : "memory");
}
struct FragA { bf16x4 v[2][4]; };
__device__ __forceinline__ bf16x8 cat4(bf16x4 a, bf16x4 b) { return bf16x8{a[0], a[1], a[2], a[3], b[0], b[1], b[2], b[3]}; }

#define CP_STRIDE 2192

__device__ __forceinline__ void team_barrier(unsigned* cnt, unsigned target, int lane) {
  asm volatile("s_waitcnt lgkmcnt(0)" ::: "memory");
  if (lane == 0) __hip_atomic_fetch_add(cnt, 1u, __ATOMIC_RELAXED, __HIP_MEMORY_SCOPE_WORKGROUP);
  while (true) {
    unsigned v = __hip_atomic_load(cnt, __ATOMIC_RELAXED, __HIP_MEMORY_SCOPE_WORKGROUP);
    if (__builtin_amdgcn_readfirstlane(v) >= target) break;
    __builtin_amdgcn_s_sleep(1);
  }
  asm volatile("" ::: "memory");
}

#define NS1 3
#define CONV_OFF (NS1 * STAGE_BYTES)
__global__ __launch_bounds__(512) void mid_kernel(const u16* __restrict__ h, const u16* __restrict__ w1,
                                                  const u16* __restrict__ b1, u16* __restrict__ G,
                                                  const u16* __restrict__ xnT, const float* __restrict__ Kdg,
                                                  u16* __restrict__ yT) {
  __shared__ __attribute__((aligned(16))) char smem[CONV_OFF + 35072 + 15360 + 2592 + 32];
  const int lane = threadIdx.x & 63;
  unsigned* cnts = (unsigned*)(smem + CONV_OFF + 35072 + 15360 + 2592);
  if (threadIdx.x < 8) cnts[threadIdx.x] = 0u;
  __syncthreads();
  if (threadIdx.x < 256) {
    if (blockIdx.x >= 224) return;
    const int tid = threadIdx.x, wave = tid >> 6;
    const int tile = (blockIdx.x & 7) * 28 + (blockIdx.x >> 3);
    const int bm = tile & 7, bn = tile >> 3;
    const int m0 = bm * 64, n0 = bn * 64;
    f32x4 acc[4][4] = {};
    const u16* srcA[4]; const u16* srcB[4];
#pragma unroll
    for (int i = 0; i < 4; ++i) {
      const unsigned row = wave * 32 + i * 8 + (lane >> 3);
      const unsigned sc = kslow_src_chunk(row, lane & 7);
      srcA[i] = h + (size_t)row * 512 + m0 + sc * 8;
      srcB[i] = w1 + (size_t)row * 1792 + n0 + sc * 8;
    }
    auto issue = [&](int t) {
      char* st = smem + (t % NS1) * STAGE_BYTES;
#pragma unroll
      for (int i = 0; i < 4; ++i) {
        GLDS(srcA[i] + (size_t)t * 128 * 512, st + (wave * 32 + i * 8) * 128);
        GLDS(srcB[i] + (size_t)t * 128 * 1792, st + 16384 + (wave * 32 + i * 8) * 128);
      }
    };
    const int NT = 16;
    const bf16x8* b1p = (const bf16x8*)(b1 + n0 + (tid & 3) * 16);
    const bf16x8 b1v[2] = {b1p[0], b1p[1]};
    asm volatile("" ::: "memory");
#pragma unroll
    for (int s = 0; s < NS1; ++s) issue(s);
    const unsigned g = lane >> 4, q = (lane & 15) >> 2, p = lane & 3;
    const unsigned r0 = wave * 32 + 8 * g + q;
    struct F8 { bf16x4 v[8]; };
    F8 fa[2], fb[2];
    auto load_half = [&](F8& A, unsigned base, bool lim) {
      tr_read8(base + swz_off(r0, 4 * p), base + swz_off(r0 + 4, 4 * p),
               base + swz_off(r0, 16 + 4 * p), base + swz_off(r0 + 4, 16 + 4 * p), A.v[0], A.v[1], A.v[2], A.v[3]);
      if (lim) asm volatile("s_waitcnt lgkmcnt(8)" ::: "memory");
      tr_read8(base + swz_off(r0, 32 + 4 * p), base + swz_off(r0 + 4, 32 + 4 * p),
               base + swz_off(r0, 48 + 4 * p), base + swz_off(r0 + 4, 48 + 4 * p), A.v[4], A.v[5], A.v[6], A.v[7]);
    };
    auto compute = [&](F8& A, F8& B) {
#pragma unroll
      for (int i = 0; i < 4; ++i) {
        bf16x8 a = cat4(A.v[2 * i], A.v[2 * i + 1]);
#pragma unroll
        for (int j = 0; j < 4; ++j)
          acc[i][j] = __builtin_amdgcn_mfma_f32_16x16x32_bf16(a, cat4(B.v[2 * j], B.v[2 * j + 1]), acc[i][j], 0, 0, 0);
      }
    };
    auto step = [&](int t, F8& Ac, F8& Bc, F8& An, F8& Bn) {
      if (t + 1 < NT) {
        if (t + 2 < NT) WAIT_VM(8); else WAIT_VM(0);
        asm volatile("s_waitcnt lgkmcnt(0)" ::: "memory");
        if (t + 3 < NT) issue(t + 3);
        const unsigned base = (unsigned)(size_t)(smem + ((t + 1) % NS1) * STAGE_BYTES);
        load_half(An, base, false);
        load_half(Bn, base + 16384, true);
      } else {
        asm volatile("s_waitcnt lgkmcnt(0)" ::: "memory");
      }
      __builtin_amdgcn_sched_barrier(0);
      compute(Ac, Bc);
      __builtin_amdgcn_sched_barrier(0);
    };
    WAIT_VM(16);
    load_half(fa[0], (unsigned)(size_t)smem, false);
    load_half(fb[0], (unsigned)(size_t)smem + 16384, true);
    for (int t = 0; t < NT; t += 2) {
      step(t, fa[0], fb[0], fa[1], fb[1]);
      step(t + 1, fa[1], fb[1], fa[0], fb[0]);
    }
    team_barrier(cnts, 4u, lane);
    float* red = (float*)smem;
#pragma unroll
    for (int i = 0; i < 4; ++i)
#pragma unroll
      for (int j = 0; j < 4; ++j)
#pragma unroll
        for (int r = 0; r < 4; ++r)
          red[wave * 4352 + (16 * i + (lane >> 4) * 4 + r) * 68 + 16 * j + (lane & 15)] = acc[i][j][r];
    team_barrier(cnts, 8u, lane);
    {
      const int row = tid >> 2, c0 = (tid & 3) * 16;
      bf16x8 o[2];
#pragma unroll
      for (int c4 = 0; c4 < 4; ++c4) {
        const f32x4* rp = (const f32x4*)(red + row * 68 + c0 + c4 * 4);
        const f32x4 v4 = (rp[0] + rp[1088]) + (rp[2 * 1088] + rp[3 * 1088]);
#pragma unroll
        for (int e = 0; e < 4; ++e) {
          const int c = c4 * 4 + e;
          float u = rbf(rbf(v4[e]) + bf2f((u16)b1v[c >> 3][c & 7]));
          o[c >> 3][c & 7] = (short)f2bf(gelu_tanh(u));
        }
      }
      bf16x8* dst = (bf16x8*)(G + (size_t)(m0 + row) * 1792 + n0 + c0);
      dst[0] = o[0]; dst[1] = o[1];
    }
    return;
  }
  typedef __attribute__((ext_vector_type(4))) unsigned u32x4;
  const int tid = threadIdx.x - 256, wave = tid >> 6;
  unsigned* cnt = cnts + 4;
  u16* cp = (u16*)(smem + CONV_OFF);
  u16* Khi = (u16*)(smem + CONV_OFF + 35072);
  u16* Klo = (u16*)(smem + CONV_OFF + 35072 + 7680);
  float* red = (float*)cp;
  unsigned phase = 0;
  u32x4 w0[2], w1v[2];
  float kv[2][8];
  u16 padv[2];
  const int pk = tid >> 5, pxx = (tid & 31) - 32;
#pragma unroll
  for (int ch = 0; ch < 2; ++ch) {
    const int d = blockIdx.x * 2 + ch;
    const u16* up = xnT + (size_t)d * 2048;
    w0[ch] = *(const u32x4*)(up + tid * 8);
    w1v[ch] = u32x4{0u, 0u, 0u, 0u};
    if (tid < 255) w1v[ch] = *(const u32x4*)(up + tid * 8 + 8);
    padv[ch] = (pxx + pk >= 0) ? up[pxx + pk] : (u16)0;
#pragma unroll
    for (int e = 0; e < 8; ++e) {
      const int l = (tid >> 2) * 32 + 31 - ((tid & 3) * 8 + e);
      kv[ch][e] = Kdg[d * 648 + idx_of_l(l)];
    }
  }
  if (tid < 128) {
    const int c2 = tid >> 2, w8 = (tid & 3) * 8;
    bf16x8 z = {};
    *(bf16x8*)(Khi + c2 * 40 + w8) = z;
    *(bf16x8*)(Klo + c2 * 40 + w8) = z;
  }
#pragma unroll
  for (int ch = 0; ch < 2; ++ch) {
    const int d = blockIdx.x * 2 + ch;
    {
      unsigned dw[8] = {w0[ch][0], w0[ch][1], w0[ch][2], w0[ch][3], w1v[ch][0], w1v[ch][1], w1v[ch][2], w1v[ch][3]};
#pragma unroll
      for (int k = 0; k < 8; ++k) {
        u32x4 o;
#pragma unroll
        for (int j = 0; j < 4; ++j)
          o[j] = (k & 1) ? __builtin_amdgcn_alignbyte(dw[j + (k + 1) / 2], dw[j + (k - 1) / 2], 2) : dw[j + k / 2];
        *(u32x4*)(cp + k * CP_STRIDE + 32 + tid * 8) = o;
      }
      cp[pk * CP_STRIDE + 32 + pxx] = padv[ch];
      bf16x8 kh, kl;
#pragma unroll
      for (int e = 0; e < 8; ++e) {
        u16 hv = f2bf(kv[ch][e]);
        kh[e] = (short)hv;
        kl[e] = (short)f2bf(kv[ch][e] - bf2f(hv));
      }
      const int c = (tid >> 2) + 32, w8 = (tid & 3) * 8;
      *(bf16x8*)(Khi + c * 40 + w8) = kh;
      *(bf16x8*)(Klo + c * 40 + w8) = kl;
    }
    team_barrier(cnt, phase += 4, lane);
    f32x16 acc0 = {}, acc1 = {};
    {
      const int r = lane & 31, hh = lane >> 5;
      const u16* abase = cp + ((r + 1) & 7) * CP_STRIDE + 8 * ((r + 1) >> 3) + 8 * hh + 32 * wave;
      const u16* kb = Khi + (r - wave + 32) * 40 + 8 * hh;
      struct Fr { bf16x8 bh0, bh1, bl0, bl1, a0, a1, c0, c1; };
      Fr f[2];
      auto ld = [&](Fr& F, int st) {
        const int t4 = (st < 8) ? 4 * st : 4 * (st - 8) - 32;
        const u16* kp = kb - t4 * 40;
        F.bh0 = *(const bf16x8*)(kp); F.bh1 = *(const bf16x8*)(kp + 16);
        F.bl0 = *(const bf16x8*)(kp + 3840); F.bl1 = *(const bf16x8*)(kp + 3840 + 16);
        F.c0 = *(const bf16x8*)(abase + 32 * (t4 + 32)); F.c1 = *(const bf16x8*)(abase + 32 * (t4 + 32) + 16);
        if (st < 8) { F.a0 = *(const bf16x8*)(abase + 32 * t4); F.a1 = *(const bf16x8*)(abase + 32 * t4 + 16); }
      };
      ld(f[0], 0);
#pragma unroll
      for (int st = 0; st < 16; ++st) {
        if (st + 1 < 16) ld(f[(st + 1) & 1], st + 1);
        Fr& F = f[st & 1];
        acc1 = __builtin_amdgcn_mfma_f32_32x32x16_bf16(F.c0, F.bh0, acc1, 0, 0, 0);
        if (st < 8) acc0 = __builtin_amdgcn_mfma_f32_32x32x16_bf16(F.a0, F.bh0, acc0, 0, 0, 0);
        acc1 = __builtin_amdgcn_mfma_f32_32x32x16_bf16(F.c1, F.bh1, acc1, 0, 0, 0);
        if (st < 8) acc0 = __builtin_amdgcn_mfma_f32_32x32x16_bf16(F.a1, F.bh1, acc0, 0, 0, 0);
        acc1 = __builtin_amdgcn_mfma_f32_32x32x16_bf16(F.c0, F.bl0, acc1, 0, 0, 0);
        if (st < 8) acc0 = __builtin_amdgcn_mfma_f32_32x32x16_bf16(F.a0, F.bl0, acc0, 0, 0, 0);
        acc1 = __builtin_amdgcn_mfma_f32_32x32x16_bf16(F.c1, F.bl1, acc1, 0, 0, 0);
        if (st < 8) acc0 = __builtin_amdgcn_mfma_f32_32x32x16_bf16(F.a1, F.bl1, acc0, 0, 0, 0);
      }
    }
    team_barrier(cnt, phase += 4, lane);
    {
      const int i = lane & 31, hh = lane >> 5;
#pragma unroll
      for (int rg = 0; rg < 16; ++rg) {
        const int row = (rg & 3) + 8 * (rg >> 2) + 4 * hh;
        red[wave * 2112 + i * 33 + row] = acc0[rg];
        red[wave * 2112 + (32 + i) * 33 + row] = acc1[rg];
      }
    }
    team_barrier(cnt, phase += 4, lane);
    {
      bf16x8 o;
#pragma unroll
      for (int e = 0; e < 8; ++e) {
        const int l = tid * 8 + e;
        const int oo = (l >> 5) * 33 + (l & 31);
        o[e] = (short)f2bf((red[oo] + red[2112 + oo]) + (red[2 * 2112 + oo] + red[3 * 2112 + oo]));
      }
      *(bf16x8*)(yT + (size_t)d * 2048 + tid * 8) = o;
    }
    if (ch == 0) team_barrier(cnt, phase += 4, lane);
  }
}

__global__ __launch_bounds__(256) void gemm2_kernel(const u16* __restrict__ w2, const u16* __restrict__ G,
                                                    const u16* __restrict__ b2, const u16* __restrict__ x,
                                                    const u16* __restrict__ xn, const u16* __restrict__ yT,
                                                    const u16* __restrict__ dskip, u16* __restrict__ out) {
  __shared__ __attribute__((aligned(16))) char smem[NS * STAGE_BYTES + 64 * 144];
  const int tid = threadIdx.x, wave = tid >> 6, lane = tid & 63;
  const int bn = (blockIdx.x >> 3) & 7, bm = (blockIdx.x & 7) * 4 + (blockIdx.x >> 6);
  const int m0 = bm * 64, n0 = bn * 64;
  f32x4 acc[4][4] = {};
  const u16* srcA[4]; const u16* srcB[4];
#pragma unroll
  for (int i = 0; i < 4; ++i) {
    const unsigned row = wave * 32 + i * 8 + (lane >> 3);
    srcA[i] = w2 + (size_t)row * 2048 + m0 + kslow_src_chunk(row, lane & 7) * 8;
    const unsigned nr = row & 63, kh = row >> 6;
    srcB[i] = G + (size_t)(n0 + nr) * 1792 + kh * 64 + (((lane & 7) ^ (nr & 7)) * 8);
  }
  auto issue = [&](int t) {
    char* st = smem + (t % NS) * STAGE_BYTES;
#pragma unroll
    for (int i = 0; i < 4; ++i) {
      GLDS(srcA[i] + (size_t)t * 128 * 2048, st + (wave * 32 + i * 8) * 128);
      GLDS(srcB[i] + t * 128, st + 16384 + (wave * 32 + i * 8) * 128);
    }
  };
  const int NT = 14;
#pragma unroll
  for (int s = 0; s < NS - 1; ++s) issue(s);
  asm volatile("" ::: "memory");
  u16* ytile = (u16*)(smem + NS * STAGE_BYTES);
  const int erow = tid >> 2, ec0 = (tid & 3) * 16;
  const bf16x8 yv0 = *(const bf16x8*)(yT + (size_t)(n0 + (tid >> 3)) * 2048 + m0 + (tid & 7) * 8);
  const bf16x8 yv1 = *(const bf16x8*)(yT + (size_t)(n0 + 32 + (tid >> 3)) * 2048 + m0 + (tid & 7) * 8);
  const bf16x8* xp = (const bf16x8*)(x + (size_t)(m0 + erow) * 512 + n0 + ec0);
  const bf16x8* xnp = (const bf16x8*)(xn + (size_t)(m0 + erow) * 512 + n0 + ec0);
  const bf16x8* skp = (const bf16x8*)(dskip + n0 + ec0);
  const bf16x8 xv[2] = {xp[0], xp[1]}, xnv[2] = {xnp[0], xnp[1]}, skv[2] = {skp[0], skp[1]};
  const u16 bb2r = b2[m0 + erow];
  asm volatile("" ::: "memory");
  const unsigned g = lane >> 4, q = (lane & 15) >> 2, p = lane & 3;
  const unsigned r0 = wave * 32 + 8 * g + q;
  struct F8 { bf16x4 v[8]; };
  struct FB { bf16x8 v[4]; };
  F8 fa[2]; FB fb[2];
  auto loadA = [&](F8& A, unsigned base) {
    tr_read8(base + swz_off(r0, 4 * p), base + swz_off(r0 + 4, 4 * p),
             base + swz_off(r0, 16 + 4 * p), base + swz_off(r0 + 4, 16 + 4 * p), A.v[0], A.v[1], A.v[2], A.v[3]);
    tr_read8(base + swz_off(r0, 32 + 4 * p), base + swz_off(r0 + 4, 32 + 4 * p),
             base + swz_off(r0, 48 + 4 * p), base + swz_off(r0 + 4, 48 + 4 * p), A.v[4], A.v[5], A.v[6], A.v[7]);
  };
  auto loadB = [&](FB& B, unsigned base) {
    const unsigned bb = base + (wave >> 1) * 8192;
    const unsigned ch = (wave & 1) * 4 + (lane >> 4);
    const unsigned n_0 = (lane & 15), n_1 = n_0 + 16, n_2 = n_0 + 32, n_3 = n_0 + 48;
    b128_read2(bb + n_0 * 128 + ((ch ^ (n_0 & 7)) << 4), bb + n_1 * 128 + ((ch ^ (n_1 & 7)) << 4), B.v[0], B.v[1]);
    b128_read2(bb + n_2 * 128 + ((ch ^ (n_2 & 7)) << 4), bb + n_3 * 128 + ((ch ^ (n_3 & 7)) << 4), B.v[2], B.v[3]);
  };
  auto compute = [&](F8& A, FB& B) {
#pragma unroll
    for (int i = 0; i < 4; ++i) {
      bf16x8 a = cat4(A.v[2 * i], A.v[2 * i + 1]);
#pragma unroll
      for (int j = 0; j < 4; ++j)
        acc[i][j] = __builtin_amdgcn_mfma_f32_16x16x32_bf16(a, B.v[j], acc[i][j], 0, 0, 0);
    }
  };
  auto step = [&](int t, F8& Ac, FB& Bc, F8& An, FB& Bn) {
    if (t < 2) WAIT_VM(17); else if (t + NS - 1 <= NT) WAIT_VM(8); else WAIT_VM(0);
    __builtin_amdgcn_s_barrier();
    if (t + NS - 1 < NT) issue(t + NS - 1);
    if (t + 1 < NT) {
      const unsigned base = (unsigned)(size_t)(smem + ((t + 1) % NS) * STAGE_BYTES);
      asm volatile("s_waitcnt lgkmcnt(0)" ::: "memory");
      loadA(An, base);
      loadB(Bn, base + 16384);
    } else {
      asm volatile("s_waitcnt lgkmcnt(0)" ::: "memory");
    }
    __builtin_amdgcn_sched_barrier(0);
    compute(Ac, Bc);
    __builtin_amdgcn_sched_barrier(0);
  };
  WAIT_VM(25);
  __builtin_amdgcn_s_barrier();
  loadA(fa[0], (unsigned)(size_t)smem);
  loadB(fb[0], (unsigned)(size_t)smem + 16384);
  for (int t = 0; t < NT; t += 2) {
    step(t, fa[0], fb[0], fa[1], fb[1]);
    step(t + 1, fa[1], fb[1], fa[0], fb[0]);
  }
  *(bf16x8*)(ytile + (tid >> 3) * 72 + (tid & 7) * 8) = yv0;
  *(bf16x8*)(ytile + (32 + (tid >> 3)) * 72 + (tid & 7) * 8) = yv1;
  __syncthreads();
  float* red = (float*)smem;
#pragma unroll
  for (int i = 0; i < 4; ++i)
#pragma unroll
    for (int j = 0; j < 4; ++j)
#pragma unroll
      for (int r = 0; r < 4; ++r)
        red[wave * 4352 + (16 * i + (lane >> 4) * 4 + r) * 68 + 16 * j + (lane & 15)] = acc[i][j][r];
  __syncthreads();
  {
    const int row = erow, c0 = ec0;
    const int s = m0 + row;
    const float bb2 = bf2f(bb2r);
    bf16x8 o[2];
#pragma unroll
    for (int c4 = 0; c4 < 4; ++c4) {
      const f32x4* rp = (const f32x4*)(red + row * 68 + c0 + c4 * 4);
      const f32x4 v4 = (rp[0] + rp[1088]) + (rp[2 * 1088] + rp[3 * 1088]);
#pragma unroll
      for (int e = 0; e < 4; ++e) {
        const int c = c4 * 4 + e;
        float val = rbf(rbf(v4[e]) + bb2);
        float a = rbf(bf2f(ytile[(c0 + c) * 72 + row]) + rbf(bf2f((u16)xnv[c >> 3][c & 7]) * bf2f((u16)skv[c >> 3][c & 7])));
        float ov = rbf(rbf(bf2f((u16)xv[c >> 3][c & 7]) + a) + val);
        o[c >> 3][c & 7] = (short)f2bf(ov);
      }
    }
    bf16x8* dst = (bf16x8*)(out + (size_t)s * 512 + n0 + c0);
    dst[0] = o[0]; dst[1] = o[1];
  }
}

extern "C" void kernel_launch(void* const* d_in, const int* in_sizes, int n_in,
                              void* d_out, int out_size, void* d_ws, size_t ws_size,
                              hipStream_t stream) {
  const u16* x = (const u16*)d_in[0];
  const u16* lar = (const u16*)d_in[1];
  const u16* aim = (const u16*)d_in[2];
  const u16* cr = (const u16*)d_in[3];
  const u16* ci = (const u16*)d_in[4];
  const u16* logdt = (const u16*)d_in[5];
  const u16* dskip = (const u16*)d_in[6];
  const u16* w1 = (const u16*)d_in[8];
  const u16* b1 = (const u16*)d_in[9];
  const u16* w2 = (const u16*)d_in[10];
  const u16* b2 = (const u16*)d_in[11];
  u16* out = (u16*)d_out;
  char* ws = (char*)d_ws;
  const size_t SZ = (size_t)2048 * 512 * 2;
  u16* xn = (u16*)(ws);
  u16* h = (u16*)(ws + SZ);
  u16* xnT = (u16*)(ws + 2 * SZ);
  u16* yT = (u16*)(ws + 3 * SZ);
  float* Kdg = (float*)(ws + 4 * SZ);
  u16* G = (u16*)(ws + 6 * SZ);
  prep_kernel<<<1024, 256, 0, stream>>>(x, xn, h, xnT, lar, aim, cr, ci, logdt, Kdg, w1, w2);
  mid_kernel<<<256, 512, 0, stream>>>(h, w1, b1, G, xnT, Kdg, yT);
  gemm2_kernel<<<256, 256, 0, stream>>>(w2, G, b2, x, xn, yT, dskip, out);
}
```

```cpp
#include <hip/hip_runtime.h>
#include <hip/hip_bf16.h>
#include <stdint.h>

typedef __attribute__((ext_vector_type(8))) short bf16x8;
typedef __attribute__((ext_vector_type(4))) short bf16x4;
typedef __attribute__((ext_vector_type(4))) float f32x4;
typedef __attribute__((ext_vector_type(16))) float f32x16;
typedef unsigned short u16;

__device__ __forceinline__ float bf2f(u16 v) { return __uint_as_float(((unsigned)v) << 16); }
__device__ __forceinline__ u16 f2bf(float f) { return __builtin_bit_cast(u16, (__bf16)f); }
__device__ __forceinline__ float rbf(float f) { return bf2f(f2bf(f)); }
typedef __attribute__((ext_vector_type(2))) float f32x2;
__device__ __forceinline__ f32x2 cmul2(f32x2 a, f32x2 b) {
  const f32x2 t = a.xx * b;
  const f32x2 bs = {-b.y, b.x};
  return __builtin_elementwise_fma(a.yy, bs, t);
}

template <int CTRL>
__device__ __forceinline__ float dpp_add(float v) {
  return v + __int_as_float(__builtin_amdgcn_update_dpp(0, __float_as_int(v), CTRL, 0xf, 0xf, true));
}
__device__ __forceinline__ float wave_sum(float v) {
  v = dpp_add<0xB1>(v);
  v = dpp_add<0x4E>(v);
  v = dpp_add<0x141>(v);
  v = dpp_add<0x140>(v);
  const int iv = __float_as_int(v);
  const float a = __int_as_float(__builtin_amdgcn_readlane(iv, 0)), b = __int_as_float(__builtin_amdgcn_readlane(iv, 16));
  const float c = __int_as_float(__builtin_amdgcn_readlane(iv, 32)), d = __int_as_float(__builtin_amdgcn_readlane(iv, 48));
  return (a + b) + (c + d);
}

__device__ __forceinline__ void ln8x2(const float (*x)[8], float (*y)[8]) {
  float s[2], q[2], mean[2];
#pragma unroll
  for (int r = 0; r < 2; ++r) {
    s[r] = ((x[r][0] + x[r][1]) + (x[r][2] + x[r][3])) + ((x[r][4] + x[r][5]) + (x[r][6] + x[r][7]));
  }
#pragma unroll
  for (int r = 0; r < 2; ++r) mean[r] = wave_sum(s[r]) * (1.0f / 512.0f);
#pragma unroll
  for (int r = 0; r < 2; ++r) {
    q[r] = 0.f;
#pragma unroll
    for (int i = 0; i < 8; ++i) { float d = x[r][i] - mean[r]; q[r] += d * d; }
  }
#pragma unroll
  for (int r = 0; r < 2; ++r) {
    float var = wave_sum(q[r]) * (1.0f / 512.0f);
    float m_b = rbf(mean[r]);
    float v_b = rbf(var);
    float den = rbf(sqrtf(rbf(v_b + 1e-5f)));
    const float rden = 1.0f / den;
#pragma unroll
    for (int i = 0; i < 8; ++i) {
      const float nn = rbf(x[r][i] - m_b);
      const float q0 = nn * rden;
      y[r][i] = rbf(fmaf(fmaf(-q0, den, nn), rden, q0));
    }
  }
}

__device__ __forceinline__ int idx_of_l(int l) {
  int p = (int)rbf((float)l);
  if (p < 256) return p;
  if (p < 512) return 256 + ((p - 256) >> 1);
  if (p < 1024) return 384 + ((p - 512) >> 2);
  return 512 + ((p - 1024) >> 3);
}
__device__ __forceinline__ float2 cexp_acc(float re, float im) {
  float e = expf(re), s, c;
  sincosf(im, &s, &c);
  return make_float2(e * c, e * s);
}
__device__ __forceinline__ float2 cmul(float2 a, float2 b) {
  return make_float2(a.x * b.x - a.y * b.y, a.x * b.y + a.y * b.x);
}

__global__ __launch_bounds__(256) void prep_kernel(const u16* __restrict__ x, u16* __restrict__ xn,
                                                   u16* __restrict__ h, u16* __restrict__ xnT,
                                                   const u16* __restrict__ lar, const u16* __restrict__ aim,
                                                   const u16* __restrict__ cr, const u16* __restrict__ ci,
                                                   const u16* __restrict__ logdt, float* __restrict__ Kdg,
                                                   const u16* __restrict__ w1, const u16* __restrict__ w2) {
  __shared__ __attribute__((aligned(16))) char smem[4 * 8704 + 2048];
  const int tid = threadIdx.x, wave = tid >> 6, lane = tid & 63;
  const unsigned bid = blockIdx.x;
  if (bid >= 768) {
    typedef __attribute__((ext_vector_type(4))) unsigned u32x4;
    const int j = bid - 768, xc = j & 7, jj = j >> 3;
    u32x4 a[7], b[7];
#pragma unroll
    for (int i = 0; i < 7; ++i) {
      const int c = i * 256 + tid;
      const int r1 = c / 28, c1 = c - r1 * 28;
      a[i] = *(const u32x4*)(w1 + (size_t)(jj * 64 + r1) * 1792 + xc * 224 + c1 * 8);
    }
#pragma unroll
    for (int i = 0; i < 7; ++i) {
      const int c = i * 256 + tid;
      b[i] = *(const u32x4*)(w2 + (size_t)(jj * 56 + (c >> 5)) * 2048 + xc * 256 + (c & 31) * 8);
    }
#pragma unroll
    for (int i = 0; i < 7; ++i) asm volatile("" :: "v"(a[i]), "v"(b[i]));
    return;
  }
  if (bid < 256) {
    u16(*tr)[512 + 8] = (u16(*)[512 + 8])smem;
    const int s0 = ((bid & 7) * 32 + (bid >> 3)) * 8;
    bf16x8 v[2];
#pragma unroll
    for (int rr = 0; rr < 2; ++rr) v[rr] = *(const bf16x8*)(x + (size_t)(s0 + wave * 2 + rr) * 512 + lane * 8);
    float xf[2][8], a[2][8], b[2][8];
#pragma unroll
    for (int rr = 0; rr < 2; ++rr)
#pragma unroll
      for (int i = 0; i < 8; ++i) xf[rr][i] = bf2f((u16)v[rr][i]);
    ln8x2(xf, a);
    ln8x2(a, b);
#pragma unroll
    for (int rr = 0; rr < 2; ++rr) {
      const int r = wave * 2 + rr;
      const int s = s0 + r;
      bf16x8 oa, ob;
#pragma unroll
      for (int i = 0; i < 8; ++i) { oa[i] = (short)f2bf(a[rr][i]); ob[i] = (short)f2bf(b[rr][i]); }
      *(bf16x8*)(&tr[r][lane * 8]) = oa;
      *(bf16x8*)(xn + (size_t)s * 512 + lane * 8) = oa;
      *(bf16x8*)(h + (size_t)s * 512 + lane * 8) = ob;
    }
    __syncthreads();
#pragma unroll
    for (int it = 0; it < 2; ++it) {
      const int d = tid + it * 256;
      bf16x8 o;
#pragma unroll
      for (int i = 0; i < 8; ++i) o[i] = (short)tr[i][d];
      *(bf16x8*)(xnT + (size_t)d * 2048 + s0) = o;
    }
    return;
  }
  const int d = bid - 256, R = wave;
  float* tab = (float*)(smem + wave * 8704);
  f32x2* Etab = (f32x2*)(smem + 4 * 8704);
  const int half = lane >> 5, m = lane & 31;
  u16 pr[4], pc[4];
#pragma unroll
  for (int c = 0; c < 4; ++c) {
    const int n = d * 128 + c * 32 + m;
    pr[c] = cr[n]; pc[c] = ci[n];
  }
  const float dt = rbf(expf(bf2f(logdt[d])));
  {
    const int n = d * 128 + (tid & 127);
    const float st0 = (tid >> 7) ? 16.f : 1.f;
    const float ar = -rbf(expf(bf2f(lar[n]))) * dt;
    const float ai = bf2f(aim[n]) * dt;
    const float2 e = cexp_acc(ar * st0, ai * st0);
    Etab[tid] = f32x2{e.x, e.y};
  }
  __syncthreads();
  const int numrows = half ? ((R == 0) ? 16 : (R == 3 ? 9 : 8)) : 16;
  const bool usebase = half && R;
  const f32x2 sgn = {1.f, half ? -1.f : 1.f};
  float* wp = tab + half * (16 * 68) + 2 * m;
  const float* rpA = tab + 16 * 68 + (lane & 15) * 68 + (lane >> 4) * 4;
  const float* rpB = tab + (lane & 15) * 68 + (lane >> 4) * 4;
  f32x2 e1[4];
#pragma unroll
  for (int c = 0; c < 4; ++c) e1[c] = Etab[half * 128 + c * 32 + m];
  for (int k = 0; k < R; ++k) {
#pragma unroll
    for (int c = 0; c < 4; ++c) e1[c] = cmul2(e1[c], e1[c]);
  }
  f32x4 acc4[4] = {};
#pragma unroll
  for (int c = 0; c < 4; ++c) {
    const f32x2 Cc = half ? f32x2{bf2f(pr[c]) * dt, bf2f(pc[c]) * dt} : f32x2{1.f, 0.f};
    const f32x2 e2 = cmul2(e1[c], e1[c]), e4 = cmul2(e2, e2), e8 = cmul2(e4, e4);
    f32x2 p[16];
    p[0] = usebase ? cmul2(Cc, e8) : Cc;
    p[1] = cmul2(p[0], e1[c]);
#pragma unroll
    for (int r = 0; r < 2; ++r) p[2 + r] = cmul2(p[r], e2);
#pragma unroll
    for (int r = 0; r < 4; ++r) p[4 + r] = cmul2(p[r], e4);
#pragma unroll
    for (int r = 0; r < 8; ++r) p[8 + r] = cmul2(p[r], e8);
#pragma unroll
    for (int r = 0; r < 16; ++r)
      *(f32x2*)(wp + r * 68) = (r < 8 || r < numrows) ? p[r] * sgn : f32x2{0.f, 0.f};
    asm volatile("" ::: "memory");
#pragma unroll
    for (int g = 0; g < 4; ++g) {
      const f32x4 av = *(const f32x4*)(rpA + g * 16);
      const f32x4 bv = *(const f32x4*)(rpB + g * 16);
#pragma unroll
      for (int e2_ = 0; e2_ < 4; ++e2_) acc4[e2_] = __builtin_amdgcn_mfma_f32_16x16x4f32(av[e2_], bv[e2_], acc4[e2_], 0, 0, 0);
    }
    asm volatile("" ::: "memory");
  }
  const f32x4 acc = (acc4[0] + acc4[1]) + (acc4[2] + acc4[3]);
  const int nout = (R == 0) ? 256 : (R == 3 ? 129 : 128);
  const int off = (R == 0) ? 0 : 128 + 128 * R;
#pragma unroll
  for (int r = 0; r < 4; ++r) {
    const int idx = ((lane >> 4) * 4 + r) * 16 + (lane & 15);
    if (idx < nout) Kdg[d * 648 + off + idx] = 2.0f * acc[r];
  }
}

__device__ __forceinline__ unsigned kslow_src_chunk(unsigned row, unsigned c16) {
  unsigned f = ((row >> 1) & 1) | (((row >> 3) & 1) << 1);
  return ((((c16 >> 1) ^ f)) << 1) | (c16 & 1);
}
__device__ __forceinline__ unsigned swz_off(unsigned row, unsigned col) {
  unsigned c32 = col >> 4;
  unsigned f = ((row >> 1) & 1) | (((row >> 3) & 1) << 1);
  return row * 128 + ((c32 ^ f) << 5) + ((col & 15) << 1);
}
__device__ __forceinline__ float gelu_tanh(float x) {
  float k = 0.7978845608028654f * (x + 0.044715f * x * x * x);
  return x / (1.0f + expf(-2.0f * k));
}
#define GLDS(src, dst) __builtin_amdgcn_global_load_lds((const unsigned*)(src), (unsigned*)(dst), 16, 0, 0)
#define NS 4
#define STAGE_BYTES 32768
#define WAIT_VM(n) asm volatile("s_waitcnt vmcnt(" #n ")" ::: "memory")

__device__ __forceinline__ void tr_read8(unsigned a0, unsigned a1, unsigned a2, unsigned a3,
                                         bf16x4& r0, bf16x4& r1, bf16x4& r2, bf16x4& r3) {
  asm volatile("ds_read_b64_tr_b16 %0, %4\n\tds_read_b64_tr_b16 %1, %5\n\tds_read_b64_tr_b16 %2, %6\n\tds_read_b64_tr_b16 %3, %7"
               : "=&v"(r0), "=&v"(r1), "=&v"(r2), "=&v"(r3) : "v"(a0), "v"(a1), "v"(a2), "v"(a3) : "memory");
}

__device__ __forceinline__ void b128_read2(unsigned a0, unsigned a1, bf16x8& r0, bf16x8& r1) {
  asm volatile("ds_read_b128 %0, %2\n\tds_read_b128 %1, %3" : "=&v"(r0), "=&v"(r1) : "v"(a0), "v"(a1) : "memory");
}
struct FragA { bf16x4 v[2][4]; };
__device__ __forceinline__ bf16x8 cat4(bf16x4 a, bf16x4 b) { return bf16x8{a[0], a[1], a[2], a[3], b[0], b[1], b[2], b[3]}; }

#define CP_STRIDE 2192

__device__ __forceinline__ void team_barrier(unsigned* cnt, unsigned target, int lane) {
  asm volatile("s_waitcnt lgkmcnt(0)" ::: "memory");
  if (lane == 0) __hip_atomic_fetch_add(cnt, 1u, __ATOMIC_RELAXED, __HIP_MEMORY_SCOPE_WORKGROUP);
  while (true) {
    unsigned v = __hip_atomic_load(cnt, __ATOMIC_RELAXED, __HIP_MEMORY_SCOPE_WORKGROUP);
    if (__builtin_amdgcn_readfirstlane(v) >= target) break;
    __builtin_amdgcn_s_sleep(1);
  }
  asm volatile("" ::: "memory");
}

#define NS1 3
#define CONV_OFF (NS1 * STAGE_BYTES)
__global__ __launch_bounds__(512) void mid_kernel(const u16* __restrict__ h, const u16* __restrict__ w1,
                                                  const u16* __restrict__ b1, u16* __restrict__ G,
                                                  const u16* __restrict__ xnT, const float* __restrict__ Kdg,
                                                  u16* __restrict__ yT) {
  __shared__ __attribute__((aligned(16))) char smem[CONV_OFF + 35072 + 15360 + 2592 + 32];
  const int lane = threadIdx.x & 63;
  unsigned* cnts = (unsigned*)(smem + CONV_OFF + 35072 + 15360 + 2592);
  if (threadIdx.x < 8) cnts[threadIdx.x] = 0u;
  __syncthreads();
  if (threadIdx.x < 256) {
    if (blockIdx.x >= 224) return;
    const int tid = threadIdx.x, wave = tid >> 6;
    const int tile = (blockIdx.x & 7) * 28 + (blockIdx.x >> 3);
    const int bm = tile & 7, bn = tile >> 3;
    const int m0 = bm * 64, n0 = bn * 64;
    f32x4 acc[4][4] = {};
    const u16* srcA[4]; const u16* srcB[4];
#pragma unroll
    for (int i = 0; i < 4; ++i) {
      const unsigned row = wave * 32 + i * 8 + (lane >> 3);
      const unsigned sc = kslow_src_chunk(row, lane & 7);
      srcA[i] = h + (size_t)row * 512 + m0 + sc * 8;
      srcB[i] = w1 + (size_t)row * 1792 + n0 + sc * 8;
    }
    auto issue = [&](int t) {
      char* st = smem + (t % NS1) * STAGE_BYTES;
#pragma unroll
      for (int i = 0; i < 4; ++i) {
        GLDS(srcA[i] + (size_t)t * 128 * 512, st + (wave * 32 + i * 8) * 128);
        GLDS(srcB[i] + (size_t)t * 128 * 1792, st + 16384 + (wave * 32 + i * 8) * 128);
      }
    };
    const int NT = 16;
    const bf16x8* b1p = (const bf16x8*)(b1 + n0 + (tid & 3) * 16);
    const bf16x8 b1v[2] = {b1p[0], b1p[1]};
    asm volatile("" ::: "memory");
#pragma unroll
    for (int s = 0; s < NS1; ++s) issue(s);
    const unsigned g = lane >> 4, q = (lane & 15) >> 2, p = lane & 3;
    const unsigned r0 = wave * 32 + 8 * g + q;
    struct F8 { bf16x4 v[8]; };
    F8 fa[2], fb[2];
    auto load_half = [&](F8& A, unsigned base, bool lim) {
      tr_read8(base + swz_off(r0, 4 * p), base + swz_off(r0 + 4, 4 * p),
               base + swz_off(r0, 16 + 4 * p), base + swz_off(r0 + 4, 16 + 4 * p), A.v[0], A.v[1], A.v[2], A.v[3]);
      if (lim) asm volatile("s_waitcnt lgkmcnt(8)" ::: "memory");
      tr_read8(base + swz_off(r0, 32 + 4 * p), base + swz_off(r0 + 4, 32 + 4 * p),
               base + swz_off(r0, 48 + 4 * p), base + swz_off(r0 + 4, 48 + 4 * p), A.v[4], A.v[5], A.v[6], A.v[7]);
    };
    auto compute = [&](F8& A, F8& B) {
#pragma unroll
      for (int i = 0; i < 4; ++i) {
        bf16x8 a = cat4(A.v[2 * i], A.v[2 * i + 1]);
#pragma unroll
        for (int j = 0; j < 4; ++j)
          acc[i][j] = __builtin_amdgcn_mfma_f32_16x16x32_bf16(a, cat4(B.v[2 * j], B.v[2 * j + 1]), acc[i][j], 0, 0, 0);
      }
    };
    auto step = [&](int t, F8& Ac, F8& Bc, F8& An, F8& Bn) {
      if (t + 1 < NT) {
        if (t + 3 < NT) WAIT_VM(16); else if (t + 2 < NT) WAIT_VM(8); else WAIT_VM(0);
        const unsigned base = (unsigned)(size_t)(smem + ((t + 1) % NS1) * STAGE_BYTES);
        load_half(An, base, false);
        load_half(Bn, base + 16384, true);
      }
      __builtin_amdgcn_sched_barrier(0);
      compute(Ac, Bc);
      __builtin_amdgcn_sched_barrier(0);
      asm volatile("s_waitcnt lgkmcnt(0)" ::: "memory");
      if (t + 4 < NT) issue(t + 4);
    };
    WAIT_VM(16);
    load_half(fa[0], (unsigned)(size_t)smem, false);
    load_half(fb[0], (unsigned)(size_t)smem + 16384, true);
    asm volatile("s_waitcnt lgkmcnt(0)" ::: "memory");
    issue(3);
    for (int t = 0; t < NT; t += 2) {
      step(t, fa[0], fb[0], fa[1], fb[1]);
      step(t + 1, fa[1], fb[1], fa[0], fb[0]);
    }
    team_barrier(cnts, 4u, lane);
    float* red = (float*)smem;
#pragma unroll
    for (int i = 0; i < 4; ++i)
#pragma unroll
      for (int j = 0; j < 4; ++j)
#pragma unroll
        for (int r = 0; r < 4; ++r)
          red[wave * 4352 + (16 * i + (lane >> 4) * 4 + r) * 68 + 16 * j + (lane & 15)] = acc[i][j][r];
    team_barrier(cnts, 8u, lane);
    {
      const int row = tid >> 2, c0 = (tid & 3) * 16;
      bf16x8 o[2];
#pragma unroll
      for (int c4 = 0; c4 < 4; ++c4) {
        const f32x4* rp = (const f32x4*)(red + row * 68 + c0 + c4 * 4);
        const f32x4 v4 = (rp[0] + rp[1088]) + (rp[2 * 1088] + rp[3 * 1088]);
#pragma unroll
        for (int e = 0; e < 4; ++e) {
          const int c = c4 * 4 + e;
          float u = rbf(rbf(v4[e]) + bf2f((u16)b1v[c >> 3][c & 7]));
          o[c >> 3][c & 7] = (short)f2bf(gelu_tanh(u));
        }
      }
      bf16x8* dst = (bf16x8*)(G + (size_t)(m0 + row) * 1792 + n0 + c0);
      dst[0] = o[0]; dst[1] = o[1];
    }
    return;
  }
  typedef __attribute__((ext_vector_type(4))) unsigned u32x4;
  const int tid = threadIdx.x - 256, wave = tid >> 6;
  unsigned* cnt = cnts + 4;
  u16* cp = (u16*)(smem + CONV_OFF);
  u16* Khi = (u16*)(smem + CONV_OFF + 35072);
  u16* Klo = (u16*)(smem + CONV_OFF + 35072 + 7680);
  float* red = (float*)cp;
  unsigned phase = 0;
  u32x4 w0[2], w1v[2];
  float kv[2][8];
  u16 padv[2];
  const int pk = tid >> 5, pxx = (tid & 31) - 32;
#pragma unroll
  for (int ch = 0; ch < 2; ++ch) {
    const int d = blockIdx.x * 2 + ch;
    const u16* up = xnT + (size_t)d * 2048;
    w0[ch] = *(const u32x4*)(up + tid * 8);
    w1v[ch] = u32x4{0u, 0u, 0u, 0u};
    if (tid < 255) w1v[ch] = *(const u32x4*)(up + tid * 8 + 8);
    padv[ch] = (pxx + pk >= 0) ? up[pxx + pk] : (u16)0;
#pragma unroll
    for (int e = 0; e < 8; ++e) {
      const int l = (tid >> 2) * 32 + 31 - ((tid & 3) * 8 + e);
      kv[ch][e] = Kdg[d * 648 + idx_of_l(l)];
    }
  }
  if (tid < 128) {
    const int c2 = tid >> 2, w8 = (tid & 3) * 8;
    bf16x8 z = {};
    *(bf16x8*)(Khi + c2 * 40 + w8) = z;
    *(bf16x8*)(Klo + c2 * 40 + w8) = z;
  }
#pragma unroll
  for (int ch = 0; ch < 2; ++ch) {
    const int d = blockIdx.x * 2 + ch;
    {
      unsigned dw[8] = {w0[ch][0], w0[ch][1], w0[ch][2], w0[ch][3], w1v[ch][0], w1v[ch][1], w1v[ch][2], w1v[ch][3]};
#pragma unroll
      for (int k = 0; k < 8; ++k) {
        u32x4 o;
#pragma unroll
        for (int j = 0; j < 4; ++j)
          o[j] = (k & 1) ? __builtin_amdgcn_alignbyte(dw[j + (k + 1) / 2], dw[j + (k - 1) / 2], 2) : dw[j + k / 2];
        *(u32x4*)(cp + k * CP_STRIDE + 32 + tid * 8) = o;
      }
      cp[pk * CP_STRIDE + 32 + pxx] = padv[ch];
      bf16x8 kh, kl;
#pragma unroll
      for (int e = 0; e < 8; ++e) {
        u16 hv = f2bf(kv[ch][e]);
        kh[e] = (short)hv;
        kl[e] = (short)f2bf(kv[ch][e] - bf2f(hv));
      }
      const int c = (tid >> 2) + 32, w8 = (tid & 3) * 8;
      *(bf16x8*)(Khi + c * 40 + w8) = kh;
      *(bf16x8*)(Klo + c * 40 + w8) = kl;
    }
    team_barrier(cnt, phase += 4, lane);
    f32x16 acc0 = {}, acc1 = {};
    {
      const int r = lane & 31, hh = lane >> 5;
      const u16* abase = cp + ((r + 1) & 7) * CP_STRIDE + 8 * ((r + 1) >> 3) + 8 * hh + 32 * wave;
      const u16* kb = Khi + (r - wave + 32) * 40 + 8 * hh;
      struct Fr { bf16x8 bh0, bh1, bl0, bl1, a0, a1, c0, c1; };
      Fr f[2];
      auto ld = [&](Fr& F, int st) {
        const int t4 = (st < 8) ? 4 * st : 4 * (st - 8) - 32;
        const u16* kp = kb - t4 * 40;
        F.bh0 = *(const bf16x8*)(kp); F.bh1 = *(const bf16x8*)(kp + 16);
        F.bl0 = *(const bf16x8*)(kp + 3840); F.bl1 = *(const bf16x8*)(kp + 3840 + 16);
        F.c0 = *(const bf16x8*)(abase + 32 * (t4 + 32)); F.c1 = *(const bf16x8*)(abase + 32 * (t4 + 32) + 16);
        if (st < 8) { F.a0 = *(const bf16x8*)(abase + 32 * t4); F.a1 = *(const bf16x8*)(abase + 32 * t4 + 16); }
      };
      ld(f[0], 0);
#pragma unroll
      for (int st = 0; st < 16; ++st) {
        if (st + 1 < 16) ld(f[(st + 1) & 1], st + 1);
        Fr& F = f[st & 1];
        acc1 = __builtin_amdgcn_mfma_f32_32x32x16_bf16(F.c0, F.bh0, acc1, 0, 0, 0);
        if (st < 8) acc0 = __builtin_amdgcn_mfma_f32_32x32x16_bf16(F.a0, F.bh0, acc0, 0, 0, 0);
        acc1 = __builtin_amdgcn_mfma_f32_32x32x16_bf16(F.c1, F.bh1, acc1, 0, 0, 0);
        if (st < 8) acc0 = __builtin_amdgcn_mfma_f32_32x32x16_bf16(F.a1, F.bh1, acc0, 0, 0, 0);
        acc1 = __builtin_amdgcn_mfma_f32_32x32x16_bf16(F.c0, F.bl0, acc1, 0, 0, 0);
        if (st < 8) acc0 = __builtin_amdgcn_mfma_f32_32x32x16_bf16(F.a0, F.bl0, acc0, 0, 0, 0);
        acc1 = __builtin_amdgcn_mfma_f32_32x32x16_bf16(F.c1, F.bl1, acc1, 0, 0, 0);
        if (st < 8) acc0 = __builtin_amdgcn_mfma_f32_32x32x16_bf16(F.a1, F.bl1, acc0, 0, 0, 0);
      }
    }
    team_barrier(cnt, phase += 4, lane);
    {
      const int i = lane & 31, hh = lane >> 5;
#pragma unroll
      for (int rg = 0; rg < 16; ++rg) {
        const int row = (rg & 3) + 8 * (rg >> 2) + 4 * hh;
        red[wave * 2112 + i * 33 + row] = acc0[rg];
        red[wave * 2112 + (32 + i) * 33 + row] = acc1[rg];
      }
    }
    team_barrier(cnt, phase += 4, lane);
    {
      bf16x8 o;
#pragma unroll
      for (int e = 0; e < 8; ++e) {
        const int l = tid * 8 + e;
        const int oo = (l >> 5) * 33 + (l & 31);
        o[e] = (short)f2bf((red[oo] + red[2112 + oo]) + (red[2 * 2112 + oo] + red[3 * 2112 + oo]));
      }
      *(bf16x8*)(yT + (size_t)d * 2048 + tid * 8) = o;
    }
    if (ch == 0) team_barrier(cnt, phase += 4, lane);
  }
}

__global__ __launch_bounds__(256) void gemm2_kernel(const u16* __restrict__ w2, const u16* __restrict__ G,
                                                    const u16* __restrict__ b2, const u16* __restrict__ x,
                                                    const u16* __restrict__ xn, const u16* __restrict__ yT,
                                                    const u16* __restrict__ dskip, u16* __restrict__ out) {
  __shared__ __attribute__((aligned(16))) char smem[NS * STAGE_BYTES + 64 * 144];
  const int tid = threadIdx.x, wave = tid >> 6, lane = tid & 63;
  const int bn = (blockIdx.x >> 3) & 7, bm = (blockIdx.x & 7) * 4 + (blockIdx.x >> 6);
  const int m0 = bm * 64, n0 = bn * 64;
  f32x4 acc[4][4] = {};
  const u16* srcA[4]; const u16* srcB[4];
#pragma unroll
  for (int i = 0; i < 4; ++i) {
    const unsigned row = wave * 32 + i * 8 + (lane >> 3);
    srcA[i] = w2 + (size_t)row * 2048 + m0 + kslow_src_chunk(row, lane & 7) * 8;
    const unsigned nr = row & 63, kh = row >> 6;
    srcB[i] = G + (size_t)(n0 + nr) * 1792 + kh * 64 + (((lane & 7) ^ (nr & 7)) * 8);
  }
  auto issue = [&](int t) {
    char* st = smem + (t % NS) * STAGE_BYTES;
#pragma unroll
    for (int i = 0; i < 4; ++i) {
      GLDS(srcA[i] + (size_t)t * 128 * 2048, st + (wave * 32 + i * 8) * 128);
      GLDS(srcB[i] + t * 128, st + 16384 + (wave * 32 + i * 8) * 128);
    }
  };
  const int NT = 14;
#pragma unroll
  for (int s = 0; s < NS; ++s) issue(s);
  asm volatile("" ::: "memory");
  u16* ytile = (u16*)(smem + NS * STAGE_BYTES);
  const int erow = tid >> 2, ec0 = (tid & 3) * 16;
  const bf16x8 yv0 = *(const bf16x8*)(yT + (size_t)(n0 + (tid >> 3)) * 2048 + m0 + (tid & 7) * 8);
  const bf16x8 yv1 = *(const bf16x8*)(yT + (size_t)(n0 + 32 + (tid >> 3)) * 2048 + m0 + (tid & 7) * 8);
  const bf16x8* xp = (const bf16x8*)(x + (size_t)(m0 + erow) * 512 + n0 + ec0);
  const bf16x8* xnp = (const bf16x8*)(xn + (size_t)(m0 + erow) * 512 + n0 + ec0);
  const bf16x8* skp = (const bf16x8*)(dskip + n0 + ec0);
  const bf16x8 xv[2] = {xp[0], xp[1]}, xnv[2] = {xnp[0], xnp[1]}, skv[2] = {skp[0], skp[1]};
  const u16 bb2r = b2[m0 + erow];
  asm volatile("" ::: "memory");
  const unsigned g = lane >> 4, q = (lane & 15) >> 2, p = lane & 3;
  const unsigned r0 = wave * 32 + 8 * g + q;
  struct F8 { bf16x4 v[8]; };
  struct FB { bf16x8 v[4]; };
  F8 fa[2]; FB fb[2];
  auto loadA = [&](F8& A, unsigned base) {
    tr_read8(base + swz_off(r0, 4 * p), base + swz_off(r0 + 4, 4 * p),
             base + swz_off(r0, 16 + 4 * p), base + swz_off(r0 + 4, 16 + 4 * p), A.v[0], A.v[1], A.v[2], A.v[3]);
    tr_read8(base + swz_off(r0, 32 + 4 * p), base + swz_off(r0 + 4, 32 + 4 * p),
             base + swz_off(r0, 48 + 4 * p), base + swz_off(r0 + 4, 48 + 4 * p), A.v[4], A.v[5], A.v[6], A.v[7]);
  };
  auto loadB = [&](FB& B, unsigned base) {
    const unsigned bb = base + (wave >> 1) * 8192;
    const unsigned ch = (wave & 1) * 4 + (lane >> 4);
    const unsigned n_0 = (lane & 15), n_1 = n_0 + 16, n_2 = n_0 + 32, n_3 = n_0 + 48;
    b128_read2(bb + n_0 * 128 + ((ch ^ (n_0 & 7)) << 4), bb + n_1 * 128 + ((ch ^ (n_1 & 7)) << 4), B.v[0], B.v[1]);
    b128_read2(bb + n_2 * 128 + ((ch ^ (n_2 & 7)) << 4), bb + n_3 * 128 + ((ch ^ (n_3 & 7)) << 4), B.v[2], B.v[3]);
  };
  auto compute = [&](F8& A, FB& B) {
#pragma unroll
    for (int i = 0; i < 4; ++i) {
      bf16x8 a = cat4(A.v[2 * i], A.v[2 * i + 1]);
#pragma unroll
      for (int j = 0; j < 4; ++j)
        acc[i][j] = __builtin_amdgcn_mfma_f32_16x16x32_bf16(a, B.v[j], acc[i][j], 0, 0, 0);
    }
  };
  auto step = [&](int t, F8& Ac, FB& Bc, F8& An, FB& Bn) {
    asm volatile("s_waitcnt lgkmcnt(0)" ::: "memory");
    if (t < 3) WAIT_VM(25); else if (t + 3 < NT) WAIT_VM(16); else if (t + 2 < NT) WAIT_VM(8); else WAIT_VM(0);
    __builtin_amdgcn_s_barrier();
    if (t + NS < NT) issue(t + NS);
    if (t + 1 < NT) {
      const unsigned base = (unsigned)(size_t)(smem + ((t + 1) % NS) * STAGE_BYTES);
      loadA(An, base);
      loadB(Bn, base + 16384);
    }
    __builtin_amdgcn_sched_barrier(0);
    compute(Ac, Bc);
    __builtin_amdgcn_sched_barrier(0);
  };
  WAIT_VM(33);
  __builtin_amdgcn_s_barrier();
  loadA(fa[0], (unsigned)(size_t)smem);
  loadB(fb[0], (unsigned)(size_t)smem + 16384);
  for (int t = 0; t < NT; t += 2) {
    step(t, fa[0], fb[0], fa[1], fb[1]);
    step(t + 1, fa[1], fb[1], fa[0], fb[0]);
  }
  *(bf16x8*)(ytile + (tid >> 3) * 72 + (tid & 7) * 8) = yv0;
  *(bf16x8*)(ytile + (32 + (tid >> 3)) * 72 + (tid & 7) * 8) = yv1;
  __syncthreads();
  float* red = (float*)smem;
#pragma unroll
  for (int i = 0; i < 4; ++i)
#pragma unroll
    for (int j = 0; j < 4; ++j)
#pragma unroll
      for (int r = 0; r < 4; ++r)
        red[wave * 4352 + (16 * i + (lane >> 4) * 4 + r) * 68 + 16 * j + (lane & 15)] = acc[i][j][r];
  __syncthreads();
  {
    const int row = erow, c0 = ec0;
    const int s = m0 + row;
    const float bb2 = bf2f(bb2r);
    bf16x8 o[2];
#pragma unroll
    for (int c4 = 0; c4 < 4; ++c4) {
      const f32x4* rp = (const f32x4*)(red + row * 68 + c0 + c4 * 4);
      const f32x4 v4 = (rp[0] + rp[1088]) + (rp[2 * 1088] + rp[3 * 1088]);
#pragma unroll
      for (int e = 0; e < 4; ++e) {
        const int c = c4 * 4 + e;
        float val = rbf(rbf(v4[e]) + bb2);
        float a = rbf(bf2f(ytile[(c0 + c) * 72 + row]) + rbf(bf2f((u16)xnv[c >> 3][c & 7]) * bf2f((u16)skv[c >> 3][c & 7])));
        float ov = rbf(rbf(bf2f((u16)xv[c >> 3][c & 7]) + a) + val);
        o[c >> 3][c & 7] = (short)f2bf(ov);
      }
    }
    bf16x8* dst = (bf16x8*)(out + (size_t)s * 512 + n0 + c0);
    dst[0] = o[0]; dst[1] = o[1];
  }
}

extern "C" void kernel_launch(void* const* d_in, const int* in_sizes, int n_in,
                              void* d_out, int out_size, void* d_ws, size_t ws_size,
                              hipStream_t stream) {
  const u16* x = (const u16*)d_in[0];
  const u16* lar = (const u16*)d_in[1];
  const u16* aim = (const u16*)d_in[2];
  const u16* cr = (const u16*)d_in[3];
  const u16* ci = (const u16*)d_in[4];
  const u16* logdt = (const u16*)d_in[5];
  const u16* dskip = (const u16*)d_in[6];
  const u16* w1 = (const u16*)d_in[8];
  const u16* b1 = (const u16*)d_in[9];
  const u16* w2 = (const u16*)d_in[10];
  const u16* b2 = (const u16*)d_in[11];
  u16* out = (u16*)d_out;
  char* ws = (char*)d_ws;
  const size_t SZ = (size_t)2048 * 512 * 2;
  u16* xn = (u16*)(ws);
  u16* h = (u16*)(ws + SZ);
  u16* xnT = (u16*)(ws + 2 * SZ);
  u16* yT = (u16*)(ws + 3 * SZ);
  float* Kdg = (float*)(ws + 4 * SZ);
  u16* G = (u16*)(ws + 6 * SZ);
  prep_kernel<<<1024, 256, 0, stream>>>(x, xn, h, xnT, lar, aim, cr, ci, logdt, Kdg, w1, w2);
  mid_kernel<<<256, 512, 0, stream>>>(h, w1, b1, G, xnT, Kdg, yT);
  gemm2_kernel<<<256, 256, 0, stream>>>(w2, G, b2, x, xn, yT, dskip, out);
}
```

```cpp
#define NB2 1
#define GSC " sc1"
#define PSL 1
#include <hip/hip_runtime.h>
#include <hip/hip_bf16.h>
#include <stdint.h>

typedef __attribute__((ext_vector_type(8))) short bf16x8;
typedef __attribute__((ext_vector_type(4))) short bf16x4;
typedef __attribute__((ext_vector_type(4))) float f32x4;
typedef __attribute__((ext_vector_type(16))) float f32x16;
typedef unsigned short u16;
#define STB(base, kern)
#define STAMP(c, slot)
#define STAMPW(c, slot)
typedef __attribute__((ext_vector_type(2))) __bf16 bf16x2;
typedef __attribute__((ext_vector_type(2))) unsigned u32x2;
typedef __attribute__((ext_vector_type(4))) unsigned u32x4;

__device__ __forceinline__ float bf2f(u16 v) { return __uint_as_float(((unsigned)v) << 16); }
__device__ __forceinline__ u16 f2bf(float f) { return __builtin_bit_cast(u16, (__bf16)f); }
__device__ __forceinline__ float rbf(float f) { return bf2f(f2bf(f)); }
typedef __attribute__((ext_vector_type(2))) float f32x2;
__device__ __forceinline__ f32x2 cmul2(f32x2 a, f32x2 b) {
  f32x2 r;
  asm("v_pk_mul_f32 %0, %1, %2 op_sel:[0,0] op_sel_hi:[0,1]\n\t"
      "v_pk_fma_f32 %0, %1, %2, %0 op_sel:[1,1,0] op_sel_hi:[1,0,1] neg_lo:[0,1,0]" : "=&v"(r) : "v"(a), "v"(b));
  return r;
}

template <int CTRL>
__device__ __forceinline__ float dpp_add(float v) {
  return v + __int_as_float(__builtin_amdgcn_update_dpp(0, __float_as_int(v), CTRL, 0xf, 0xf, true));
}
__device__ __forceinline__ float wave_sum(float v) {
  v = dpp_add<0xB1>(v);
  v = dpp_add<0x4E>(v);
  v = dpp_add<0x141>(v);
  v = dpp_add<0x140>(v);
  const int iv = __float_as_int(v);
  const float a = __int_as_float(__builtin_amdgcn_readlane(iv, 0)), b = __int_as_float(__builtin_amdgcn_readlane(iv, 16));
  const float c = __int_as_float(__builtin_amdgcn_readlane(iv, 32)), d = __int_as_float(__builtin_amdgcn_readlane(iv, 48));
  return (a + b) + (c + d);
}

__device__ __forceinline__ void ln8x2(const float (*x)[8], float (*y)[8]) {
  float s[2], q[2], mean[2];
#pragma unroll
  for (int r = 0; r < 2; ++r) {
    s[r] = ((x[r][0] + x[r][1]) + (x[r][2] + x[r][3])) + ((x[r][4] + x[r][5]) + (x[r][6] + x[r][7]));
  }
#pragma unroll
  for (int r = 0; r < 2; ++r) mean[r] = wave_sum(s[r]) * (1.0f / 512.0f);
#pragma unroll
  for (int r = 0; r < 2; ++r) {
    q[r] = 0.f;
#pragma unroll
    for (int i = 0; i < 8; ++i) { float d = x[r][i] - mean[r]; q[r] += d * d; }
  }
#pragma unroll
  for (int r = 0; r < 2; ++r) {
    float var = wave_sum(q[r]) * (1.0f / 512.0f);
    float m_b = rbf(mean[r]);
    float v_b = rbf(var);
    float den = rbf(sqrtf(rbf(v_b + 1e-5f)));
    const float rden = 1.0f / den;
#pragma unroll
    for (int i = 0; i < 8; ++i) {
      const float nn = rbf(x[r][i] - m_b);
      const float q0 = nn * rden;
      y[r][i] = rbf(fmaf(fmaf(-q0, den, nn), rden, q0));
    }
  }
}

__device__ __forceinline__ int idx_of_l(int l) {
  const int b = (int)f2bf((float)l) - 17024;
  return l < 128 ? l : b;
}
__device__ __forceinline__ float2 cexp_acc(float re, float im) {
  float e = expf(re), s, c;
  sincosf(im, &s, &c);
  return make_float2(e * c, e * s);
}
__device__ __forceinline__ void sincos4(const float* a, float* s, float* c) {
#pragma unroll
  for (int i = 0; i < 4; ++i) sincosf(a[i], &s[i], &c[i]);
}
__device__ __forceinline__ float swap32(float v) { return __shfl_xor(v, 32); }
#define KG_PRIO 1
#define GM_PRIO 3
#define CV_PRIO 0
__device__ __forceinline__ float2 cmul(float2 a, float2 b) {
  return make_float2(a.x * b.x - a.y * b.y, a.x * b.y + a.y * b.x);
}

#define CODE_LINES 256
#define PREP_LINES 74
#define FLS 64
#define HT_PITCH 2112
#define G_PITCH 1856
__global__ __launch_bounds__(256) void prep_kernel(const u16* __restrict__ x,
                                                   u16* __restrict__ hT, u16* __restrict__ xnT,
                                                   const u16* __restrict__ w1, const u16* __restrict__ w2,
                                                   const u16* __restrict__ lar, const u16* __restrict__ aim,
                                                   const u16* __restrict__ cr, const u16* __restrict__ ci,
                                                   const u16* __restrict__ logdt,
                                                   f32x4* __restrict__ EX, f32x2* __restrict__ EC,
                                                   unsigned* __restrict__ flags) {
  __shared__ __attribute__((aligned(16))) char smem[16 * 520 * 2];
  const int tid = threadIdx.x, wave = tid >> 6, lane = tid & 63;
  const unsigned bid = blockIdx.x;
  STB(hT, 0);
  STAMP(tid == 0, 0);
  unsigned cwarm = 0u;
  if ((bid & 255) < 32 && tid < PREP_LINES) {
    size_t pc0;
    asm volatile("s_getpc_b64 %0" : "=s"(pc0));
    cwarm = *(const __attribute__((address_space(1))) unsigned*)((pc0 & ~(size_t)127) + tid * 128);
  }
  if (bid >= 256) {
    const int j = bid - 256, xc = j & 7, jj = j >> 3;
    const int n = j * 256 + tid;
    const u16 r_lar = lar[n], r_aim = aim[n], r_cr = cr[n], r_ci = ci[n], r_dt = logdt[n >> 7];
    __builtin_amdgcn_sched_barrier(0);
    __builtin_amdgcn_s_sleep(10);
    __builtin_amdgcn_sched_barrier(0);
    u32x4 a[7], b[NB2];
#pragma unroll
    for (int i = 0; i < 7; ++i) {
      const int c = i * 256 + tid;
      const int r1 = c / 28, c1 = c - r1 * 28;
      a[i] = *(const u32x4*)(w1 + (size_t)(jj * 64 + r1) * 1792 + xc * 224 + c1 * 8);
    }
#pragma unroll
    for (int i = 0; i < NB2; ++i) {
      const int c = i * 256 + tid;
      b[i] = *(const u32x4*)(w2 + (size_t)(jj * 8 * NB2 + (c >> 5)) * 2048 + xc * 256 + (c & 31) * 8);
    }
    if (j < 2) flags[(j * 256 + tid) * FLS] = 0u;
    unsigned cw2 = 0u;
    if (tid < 8 && jj * 8 + tid < CODE_LINES) {
      size_t mk;
      asm volatile("s_getpc_b64 vcc\n\ts_add_u32 vcc_lo, vcc_lo, mid_kernel@rel32@lo+4\n\ts_addc_u32 vcc_hi, vcc_hi, mid_kernel@rel32@hi+12\n\ts_mov_b64 %0, vcc"
                   : "=s"(mk) :: "scc", "vcc");
      cw2 = *(const __attribute__((address_space(1))) unsigned*)(mk + (jj * 8 + tid) * 128);
    }
    __builtin_amdgcn_sched_barrier(0);
    {
      const float dt = rbf(expf(bf2f(r_dt)));
      const float ar = -rbf(expf(bf2f(r_lar))) * dt;
      const float ai = bf2f(r_aim) * dt;
      const float re[4] = {ar, ar * 8.f, ar * 128.f, ar * 1024.f};
      const float im[4] = {ai, ai * -8.f, ai * 128.f, ai * 1024.f};
      float ex[4], sn[4], cs[4];
#pragma unroll
      for (int i = 0; i < 4; ++i) ex[i] = expf(re[i]);
      sincos4(im, sn, cs);
      EX[2 * n] = f32x4{ex[0] * cs[0], ex[0] * sn[0], ex[1] * cs[1], ex[1] * sn[1]};
      EX[2 * n + 1] = f32x4{ex[2] * cs[2], ex[2] * sn[2], ex[3] * cs[3], ex[3] * sn[3]};
      EC[n] = f32x2{bf2f(r_cr) * dt, bf2f(r_ci) * dt};
    }
#pragma unroll
    for (int i = 0; i < 7; ++i) asm volatile("" :: "v"(a[i]), "v"(b[i % NB2]));
    asm volatile("" :: "v"(cwarm), "v"(cw2));
    STAMPW(tid < 64, 1);
    return;
  }
  if (bid < 256) {
    u16(*tr)[512 + 8] = (u16(*)[512 + 8])smem;
    const int s0 = ((bid & 7) * 32 + (bid >> 3)) * 8;
    bf16x8 v[2];
#pragma unroll
    for (int rr = 0; rr < 2; ++rr) v[rr] = *(const bf16x8*)(x + (size_t)(s0 + wave * 2 + rr) * 512 + lane * 8);
    float xf[2][8], a[2][8], b[2][8];
#pragma unroll
    for (int rr = 0; rr < 2; ++rr)
#pragma unroll
      for (int i = 0; i < 8; ++i) xf[rr][i] = bf2f((u16)v[rr][i]);
    ln8x2(xf, a);
    ln8x2(a, b);
#pragma unroll
    for (int rr = 0; rr < 2; ++rr) {
      const int r = wave * 2 + rr;
      bf16x8 oa, ob;
#pragma unroll
      for (int i = 0; i < 8; ++i) { oa[i] = (short)f2bf(a[rr][i]); ob[i] = (short)f2bf(b[rr][i]); }
      *(bf16x8*)(&tr[r][lane * 8]) = oa;
      *(bf16x8*)(&tr[8 + r][lane * 8]) = ob;
    }
    __syncthreads();
#pragma unroll
    for (int it = 0; it < 2; ++it) {
      const int d = tid + it * 256;
      bf16x8 o, o2;
#pragma unroll
      for (int i = 0; i < 8; ++i) { o[i] = (short)tr[i][d]; o2[i] = (short)tr[8 + i][d]; }
      const int li = (d & 15) + 16 * ((s0 >> 3) & 3);
      *(bf16x8*)(hT + (size_t)((d >> 4) * 64 + (s0 >> 5)) * 1024 + li * 8 + (li >= 24 ? 64 : 0)) = o2;
      *(bf16x8*)(xnT + (size_t)d * 2048 + s0) = o;
    }
    asm volatile("" :: "v"(cwarm));
    STAMPW(tid < 64, 1);
    return;
  }
}

__device__ __forceinline__ unsigned kslow_src_chunk(unsigned row, unsigned c16) {
  unsigned f = ((row >> 1) & 1) | (((row >> 3) & 1) << 1);
  return ((((c16 >> 1) ^ f)) << 1) | (c16 & 1);
}
__device__ __forceinline__ unsigned swz_off(unsigned row, unsigned col) {
  unsigned c32 = col >> 4;
  unsigned f = ((row >> 1) & 1) | (((row >> 3) & 1) << 1);
  return row * 128 + ((c32 ^ f) << 5) + ((col & 15) << 1);
}
__device__ __forceinline__ float gelu_tanh(float x) {
  float k = 0.7978845608028654f * (x + 0.044715f * x * x * x);
  return x * __builtin_amdgcn_rcpf(1.0f + __builtin_amdgcn_exp2f(-2.8853900817779268f * k));
}
#define GLDS(src, dst) __builtin_amdgcn_global_load_lds((const unsigned*)(src), (unsigned*)(dst), 16, 0, 0)
#define NS 4
#define STAGE_BYTES 32768
#define WAIT_VM(n) asm volatile("s_waitcnt vmcnt(" #n ")" ::: "memory")

__device__ __forceinline__ void tr_read8(unsigned a0, unsigned a1, unsigned a2, unsigned a3,
                                         bf16x4& r0, bf16x4& r1, bf16x4& r2, bf16x4& r3) {
  asm volatile("ds_read_b64_tr_b16 %0, %4\n\tds_read_b64_tr_b16 %1, %5\n\tds_read_b64_tr_b16 %2, %6\n\tds_read_b64_tr_b16 %3, %7"
               : "=&v"(r0), "=&v"(r1), "=&v"(r2), "=&v"(r3) : "v"(a0), "v"(a1), "v"(a2), "v"(a3) : "memory");
}

__device__ __forceinline__ void b128_read2(unsigned a0, unsigned a1, bf16x8& r0, bf16x8& r1) {
  asm volatile("ds_read_b128 %0, %2\n\tds_read_b128 %1, %3" : "=&v"(r0), "=&v"(r1) : "v"(a0), "v"(a1) : "memory");
}
struct FragA { bf16x4 v[2][4]; };
__device__ __forceinline__ bf16x8 cat4(bf16x4 a, bf16x4 b) { return bf16x8{a[0], a[1], a[2], a[3], b[0], b[1], b[2], b[3]}; }

#define ROT1 0
#define ROT2 0
#define CP_STRIDE 2080
#define KR_LEN 2304

__device__ __forceinline__ void team_barrier(unsigned* cnt, unsigned target, int lane) {
  asm volatile("s_waitcnt lgkmcnt(0)" ::: "memory");
  if (lane == 0) __hip_atomic_fetch_add(cnt, 1u, __ATOMIC_RELAXED, __HIP_MEMORY_SCOPE_WORKGROUP);
  while (true) {
    unsigned v = __hip_atomic_load(cnt, __ATOMIC_RELAXED, __HIP_MEMORY_SCOPE_WORKGROUP);
    if (__builtin_amdgcn_readfirstlane(v) >= target) break;
    __builtin_amdgcn_s_sleep(1);
  }
  asm volatile("" ::: "memory");
}
__device__ __forceinline__ void team_arrive(unsigned* cnt, int lane) {
  asm volatile("s_waitcnt lgkmcnt(0)" ::: "memory");
  if (lane == 0) __hip_atomic_fetch_add(cnt, 1u, __ATOMIC_RELAXED, __HIP_MEMORY_SCOPE_WORKGROUP);
}
__device__ __forceinline__ void team_wait(unsigned* cnt, unsigned target) {
  while (true) {
    unsigned v = __hip_atomic_load(cnt, __ATOMIC_RELAXED, __HIP_MEMORY_SCOPE_WORKGROUP);
    if (__builtin_amdgcn_readfirstlane(v) >= target) break;
    __builtin_amdgcn_s_sleep(1);
  }
  asm volatile("" ::: "memory");
}
template <int SL>
__device__ __forceinline__ void poll_flags(const unsigned* fp, bool active) {
  unsigned v = active ? 0u : 1u;
#pragma unroll 1
  for (int it = 0; it < (1 << 17); ++it) {
    if (v == 0u) v = __hip_atomic_load(fp, __ATOMIC_RELAXED, __HIP_MEMORY_SCOPE_AGENT);
    if (__builtin_amdgcn_ballot_w64(v == 0u) == 0ull) break;
    __builtin_amdgcn_s_sleep(SL);
  }
}
__device__ __forceinline__ unsigned lds_inc(unsigned* cnt) {
  unsigned old;
  asm volatile("ds_add_rtn_u32 %0, %1, %2\n\ts_waitcnt lgkmcnt(0)" : "=&v"(old) : "v"((unsigned)(size_t)cnt), "v"(1u) : "memory");
  return old;
}
__device__ __forceinline__ void st8_agent(u16* p, bf16x4 v) {
  __hip_atomic_store((unsigned long long*)p, __builtin_bit_cast(unsigned long long, v), __ATOMIC_RELAXED, __HIP_MEMORY_SCOPE_AGENT);
}
struct F8 { bf16x4 v[8]; };
__device__ __forceinline__ void gload16(bf16x8& dst, unsigned voff, const void* sbase) {
  asm volatile("global_load_dwordx4 %0, %1, %2" : "=&v"(dst) : "v"(voff), "s"(sbase) : "memory");
}
#define ACQ_G asm volatile("" ::: "memory")
__device__ __forceinline__ void gload16g(bf16x8& dst, unsigned voff, const void* sbase) {
  asm volatile("global_load_dwordx4 %0, %1, %2" GSC : "=&v"(dst) : "v"(voff), "s"(sbase) : "memory");
}
__device__ __forceinline__ void load_frag8(F8& A, unsigned base, unsigned r0, unsigned p) {
  tr_read8(base + swz_off(r0, 4 * p), base + swz_off(r0 + 4, 4 * p),
           base + swz_off(r0, 16 + 4 * p), base + swz_off(r0 + 4, 16 + 4 * p), A.v[0], A.v[1], A.v[2], A.v[3]);
  tr_read8(base + swz_off(r0, 32 + 4 * p), base + swz_off(r0 + 4, 32 + 4 * p),
           base + swz_off(r0, 48 + 4 * p), base + swz_off(r0 + 4, 48 + 4 * p), A.v[4], A.v[5], A.v[6], A.v[7]);
}
__device__ __forceinline__ f32x4 sel4(int w, f32x4 a, f32x4 b, f32x4 c, f32x4 d) {
  return w == 0 ? a : (w == 1 ? b : (w == 2 ? c : d));
}

#define NS1 3
#define SLOT 16384
#define CV_OFF (NS1 * SLOT)
#define CV_CH (8 * CP_STRIDE * 2 + 2 * KR_LEN * 2)
#define XB_OFF (CV_OFF + 2 * CV_CH)
#define XB_CH 11808
#define AT_OFF (XB_OFF + XB_CH + 8192)
#define CNT_OFF (AT_OFF + 64 * 144)
extern "C" __global__ __launch_bounds__(512) void mid_kernel(const u16* __restrict__ hT, const u16* __restrict__ w1,
                                                  const u16* __restrict__ b1, u16* G,
                                                  const u16* __restrict__ xnT,
                                                  const f32x4* __restrict__ EX, const f32x2* __restrict__ EC,
                                                  const u16* __restrict__ dskip, u16* aT,
                                                  const u16* __restrict__ w2, const u16* __restrict__ b2,
                                                  const u16* __restrict__ x, u16* __restrict__ out,
                                                  unsigned* flags, long mode) {
  __shared__ __attribute__((aligned(16))) char smem[CNT_OFF + 64];
  const int lane = threadIdx.x & 63;
  unsigned* cnts = (unsigned*)(smem + CNT_OFF);
  u16* atile = (u16*)(smem + AT_OFF);
  const int bn2 = (blockIdx.x >> 3) & 7, bm2 = (blockIdx.x & 7) * 4 + (blockIdx.x >> 6);
  const int s0 = bm2 * 64, d0 = bn2 * 64;
  STB(hT, 1);
  STAMP(threadIdx.x == 0, 0);
  asm volatile("" :: "s"(hT), "s"(w1), "s"(b1), "s"(G), "s"(xnT), "s"(EX), "s"(EC), "s"(dskip), "s"(aT), "s"(w2), "s"(b2), "s"(x), "s"(out), "s"(flags), "s"(mode));
  size_t pc0;
  asm volatile("s_getpc_b64 %0" : "=s"(pc0));
  if (threadIdx.x < 256) {
    if (threadIdx.x < 16) cnts[threadIdx.x] = 0u;
    asm volatile("s_waitcnt lgkmcnt(0)" ::: "memory");
    __builtin_amdgcn_s_barrier();
    asm volatile("" ::: "memory");
    const int tid = threadIdx.x, wave = tid >> 6;
    const unsigned p = lane & 3;
    const unsigned r0 = wave * 32 + 8 * (lane >> 4) + ((lane & 15) >> 2);
    const bool g1 = blockIdx.x < 224 && mode != 2;
    __builtin_amdgcn_s_setprio(GM_PRIO);
    const int tile = (blockIdx.x & 7) * 28 + (blockIdx.x >> 3);
    const int bm = tile & 7, bn = tile >> 3;
    unsigned tch0 = 0u;
    if (g1) {
    const int m0 = bm * 64, n0 = bn * 64;
    f32x4 acc[4][4] = {};
    const u16* srcB[4];
#pragma unroll
    for (int i = 0; i < 4; ++i) {
      const unsigned row = wave * 32 + i * 8 + (lane >> 3);
      srcB[i] = w1 + (size_t)row * 1792 + n0 + kslow_src_chunk(row, lane & 7) * 8;
    }
    const unsigned offA = ((m0 >> 4) * 64 + wave) * 2048 + lane * 16 + (lane >= 24 ? 128 : 0);
    bf16x8 ar[4][4];
    auto issue = [&](int t) {
      char* st = smem + (t % NS1) * SLOT;
#pragma unroll
      for (int i = 0; i < 4; ++i) GLDS(srcB[i] + (size_t)t * 128 * 1792, st + (wave * 32 + i * 8) * 128);
#pragma unroll
      for (int i = 0; i < 4; ++i) gload16(ar[t & 3][i], offA, hT + (i * 64 + t * 4) * 1024);
      asm volatile("" ::: "memory");
    };
    const int NT = 16;
    bf16x4 b1v[4];
#pragma unroll
    for (int j = 0; j < 4; ++j) b1v[j] = *(const bf16x4*)(b1 + n0 + 16 * j + 4 * (lane >> 4));
    asm volatile("" ::: "memory");
#pragma unroll
    for (int s = 0; s < NS1; ++s) issue(s);
    F8 fb[2];
    WAIT_VM(16);
    STAMP(tid < 64, 1);
    load_frag8(fb[0], (unsigned)(size_t)smem, r0, p);
    asm volatile("s_waitcnt lgkmcnt(0)" ::: "memory");
    issue(3);
#pragma unroll
    for (int t = 0; t < NT; ++t) {
      if (t + 1 < NT) {
        if (t + 3 < NT) WAIT_VM(16); else if (t + 2 < NT) WAIT_VM(8); else WAIT_VM(0);
        load_frag8(fb[(t + 1) & 1], (unsigned)(size_t)(smem + ((t + 1) % NS1) * SLOT), r0, p);
      }
      __builtin_amdgcn_sched_barrier(0);
#pragma unroll
      for (int j = 0; j < 4; ++j) {
        const bf16x8 b = cat4(fb[t & 1].v[2 * j], fb[t & 1].v[2 * j + 1]);
#pragma unroll
        for (int i = 0; i < 4; ++i)
          acc[j][i] = __builtin_amdgcn_mfma_f32_16x16x32_bf16(b, ar[t & 3][i], acc[j][i], 0, 0, 0);
      }
      __builtin_amdgcn_sched_barrier(0);
      asm volatile("s_waitcnt lgkmcnt(0)" ::: "memory");
      if (t + 4 < NT) issue(t + 4);
      if (t == NT - 2 && lane == 0) __hip_atomic_fetch_add(cnts + 7, 1u, __ATOMIC_RELAXED, __HIP_MEMORY_SCOPE_WORKGROUP);
    }
    {
      asm volatile("" :: "v"(b1v[0]), "v"(b1v[1]), "v"(b1v[2]), "v"(b1v[3]));
      const u16* tp = w2 + (size_t)(512 + (tid < 160 ? tid : 0) * 8 + bn2) * 2048 + s0;
      asm volatile("global_load_dword %0, %1, off" : "=&v"(tch0) : "v"(tp) : "memory");
    }
    STAMP(tid < 64, 2);
#pragma unroll
    for (int i = 0; i < 4; ++i)
      if (i != wave) {
        char* dst = smem + (i - (i > wave ? 1 : 0)) * SLOT + wave * 4096 + lane * 16;
#pragma unroll
        for (int j = 0; j < 4; ++j) *(f32x4*)(dst + j * 1024) = acc[j][i];
      }
    f32x4 own[4];
#pragma unroll
    for (int j = 0; j < 4; ++j) {
      f32x4 a0 = acc[j][0], a1 = acc[j][1], a2 = acc[j][2], a3 = acc[j][3];
      asm volatile("" : "+v"(a0), "+v"(a1), "+v"(a2), "+v"(a3));
      own[j] = sel4(wave, a0, a1, a2, a3);
    }
    team_barrier(cnts, 4u, lane);
    f32x4 xk[4][3];
#pragma unroll
    for (int kk = 0; kk < 3; ++kk) {
      const int k = kk + (kk >= wave ? 1 : 0);
      const char* src = smem + (wave - (wave > k ? 1 : 0)) * SLOT + k * 4096 + lane * 16;
#pragma unroll
      for (int j = 0; j < 4; ++j) xk[j][kk] = *(const f32x4*)(src + j * 1024);
    }
    team_arrive(cnts + 1, lane);
#pragma unroll
    for (int j = 0; j < 4; ++j) {
      const f32x4 v4 = (own[j] + xk[j][0]) + (xk[j][1] + xk[j][2]);
      bf16x4 o;
#pragma unroll
      for (int e = 0; e < 4; ++e) {
        const float u = rbf(rbf(v4[e]) + bf2f((u16)b1v[j][e]));
        o[e] = (short)f2bf(gelu_tanh(u));
      }
      const int li = (lane & 15) + 16 * (2 * (j & 1) + (lane >> 5));
      st8_agent(G + (size_t)(((m0 >> 4) + wave) * 56 + (n0 >> 5) + (j >> 1)) * 1024 + li * 8 + (li >= 24 ? 64 : 0) + 4 * ((lane >> 4) & 1), o);
    }
    asm volatile("" ::: "memory");
    if (lane == 0) __hip_atomic_fetch_add(cnts + 7, 16u, __ATOMIC_RELAXED, __HIP_MEMORY_SCOPE_WORKGROUP);
    STAMP(tid < 64, 3);
    team_wait(cnts + 1, 4u);
    }
    if (blockIdx.x >= 224 && mode == 0) {
      const int R0 = 256 * NB2, RQ = (1792 - R0) / 4;
      const u16* wq = w2 + (size_t)(R0 + ((blockIdx.x - 224) >> 3) * RQ + (tid >> 3)) * 2048 + (blockIdx.x & 7) * 256 + (tid & 7) * 32;
      unsigned tw[RQ / 32];
#pragma unroll
      for (int i = 0; i < RQ / 32; ++i) tw[i] = *(const unsigned*)(wq + (size_t)i * 32 * 2048);
#pragma unroll
      for (int i = 0; i < RQ / 32; ++i) asm volatile("" :: "v"(tw[i]));
      asm volatile("" ::: "memory");
    }
    const u16* srcA[4];
#pragma unroll
    for (int i = 0; i < 4; ++i) {
      const unsigned row = wave * 32 + i * 8 + (lane >> 3);
      srcA[i] = w2 + (size_t)row * 2048 + s0 + kslow_src_chunk(row, lane & 7) * 8;
    }
    auto issueW = [&](int t) {
      char* st = smem + (t % NS1) * SLOT;
#pragma unroll
      for (int i = 0; i < 4; ++i) GLDS(srcA[i] + (size_t)t * 128 * 2048, st + (wave * 32 + i * 8) * 128);
      asm volatile("" ::: "memory");
    };
    issueW(0); issueW(1);
    STAMP(tid < 64, 8);
    if (g1) {
      WAIT_VM(8);
      asm volatile("" :: "v"(tch0));
      if (lane == 0) {
        if (lds_inc(cnts + 2) == 3u) __hip_atomic_store(flags + (bm * 32 + bn) * FLS, 1u, __ATOMIC_RELAXED, __HIP_MEMORY_SCOPE_AGENT);
      }
      STAMP(tid < 64, 4);
    }
    asm volatile("" ::: "memory");
    issueW(2);
    const int es = s0 + 16 * wave + (lane & 15);
    bf16x4 xv[4];
#pragma unroll
    for (int j = 0; j < 4; ++j) xv[j] = *(const bf16x4*)(x + (size_t)es * 512 + d0 + 16 * j + 4 * (lane >> 4));
    const u16 bb2r = b2[es];
    asm volatile("" ::: "memory");
    if (mode == 1) { WAIT_VM(0); return; }
    F8 fa[2];
    WAIT_VM(13);
    load_frag8(fa[0], (unsigned)(size_t)smem, r0, p);
    asm volatile("s_waitcnt lgkmcnt(0)" ::: "memory");
    issueW(3);
    if (wave == 0) {
      poll_flags<PSL>(flags + (bn2 * 32 + lane) * FLS, lane < 28);
      if (lane == 0) __hip_atomic_store(cnts + 3, 1u, __ATOMIC_RELAXED, __HIP_MEMORY_SCOPE_WORKGROUP);
    } else if (g1) {
      team_wait(cnts + 3, 1u);
    } else {
      const char* cp = (const char*)(pc0 & ~(size_t)127);
      const char* pg = lane < 14 ? (const char*)G + lane * 262144 + 384
                     : (lane < 16 ? (const char*)(flags + 32) : (lane < 24 ? (const char*)aT + (lane - 16) * 262144
                     : (lane < 32 ? (const char*)out + (lane - 24) * 262144 : (lane < 40 ? (const char*)x + (lane - 32) * 262144
                     : (const char*)w2 + (lane - 40) * 294912))));
      while (true) {
        unsigned t0, t1, t2;
        asm volatile("global_load_dword %0, %3, off\n\tglobal_load_dword %1, %4, off\n\tglobal_load_dword %2, %5, off\n\ts_waitcnt vmcnt(0)"
                     : "=&v"(t0), "=&v"(t1), "=&v"(t2) : "v"(cp + (tid - 64) * 128), "v"(cp + (tid + 128 < CODE_LINES ? tid + 128 : 0) * 128), "v"(pg) : "memory");
        unsigned v = __hip_atomic_load(cnts + 3, __ATOMIC_RELAXED, __HIP_MEMORY_SCOPE_WORKGROUP);
        if (__builtin_amdgcn_readfirstlane(v) >= 1u) break;
        __builtin_amdgcn_s_sleep(8);
      }
    }
    WAIT_VM(0);
    ACQ_G;
    __builtin_amdgcn_s_setprio(1);
    STAMP(tid < 64, 10);
    const unsigned offG = ((d0 >> 4) * 56 + wave) * 2048 + lane * 16 + (lane >= 24 ? 128 : 0);
    bf16x8 gr[5][4];
    auto issueG = [&](int t) {
#pragma unroll
      for (int j = 0; j < 4; ++j) gload16g(gr[t % 5][j], offG, G + (j * 56 + t * 4) * 1024);
      asm volatile("" ::: "memory");
    };
#pragma unroll
    for (int t = 0; t < 5; ++t) issueG(t);
    f32x4 acc[4][4] = {};
    const int NT = 14;
#pragma unroll
    for (int t = 0; t < NT; ++t) {
      if (t == 0) WAIT_VM(16); else if (t == 2) WAIT_VM(24); else if (t < 10) WAIT_VM(20);
      else if (t == 10) WAIT_VM(16); else if (t == 11) WAIT_VM(8); else if (t == 12) WAIT_VM(0);
      if (t == 0) STAMP(tid < 64, 11);
      if (t + 1 < NT) load_frag8(fa[(t + 1) & 1], (unsigned)(size_t)(smem + ((t + 1) % NS1) * SLOT), r0, p);
      __builtin_amdgcn_sched_barrier(0);
#pragma unroll
      for (int i = 0; i < 4; ++i) {
        const bf16x8 a = cat4(fa[t & 1].v[2 * i], fa[t & 1].v[2 * i + 1]);
#pragma unroll
        for (int j = 0; j < 4; ++j)
          acc[j][i] = __builtin_amdgcn_mfma_f32_16x16x32_bf16(gr[t % 5][j], a, acc[j][i], 0, 0, 0);
      }
      __builtin_amdgcn_sched_barrier(0);
      asm volatile("s_waitcnt lgkmcnt(0)" ::: "memory");
      if (t + 4 < NT) issueW(t + 4);
      if (t + 5 < NT) issueG(t + 5);
    }
    STAMP(tid < 64, 12);
#pragma unroll
    for (int i = 0; i < 4; ++i)
      if (i != wave) {
        char* dst = smem + (i - (i > wave ? 1 : 0)) * SLOT + wave * 4096 + lane * 16;
#pragma unroll
        for (int j = 0; j < 4; ++j) *(f32x4*)(dst + j * 1024) = acc[j][i];
      }
    f32x4 own[4];
#pragma unroll
    for (int j = 0; j < 4; ++j) {
      f32x4 a0 = acc[j][0], a1 = acc[j][1], a2 = acc[j][2], a3 = acc[j][3];
      asm volatile("" : "+v"(a0), "+v"(a1), "+v"(a2), "+v"(a3));
      own[j] = sel4(wave, a0, a1, a2, a3);
    }
    team_barrier(cnts + 5, 5u, lane);
    STAMP(tid < 64, 13);
    const float bb2 = bf2f(bb2r);
    f32x4 xk[4][3];
#pragma unroll
    for (int kk = 0; kk < 3; ++kk) {
      const int k = kk + (kk >= wave ? 1 : 0);
      const char* src = smem + (wave - (wave > k ? 1 : 0)) * SLOT + k * 4096 + lane * 16;
#pragma unroll
      for (int j = 0; j < 4; ++j) xk[j][kk] = *(const f32x4*)(src + j * 1024);
    }
#pragma unroll
    for (int j = 0; j < 4; ++j) {
      const f32x4 v4 = (own[j] + xk[j][0]) + (xk[j][1] + xk[j][2]);
      bf16x4 o;
#pragma unroll
      for (int e = 0; e < 4; ++e) {
        const float val = rbf(rbf(v4[e]) + bb2);
        const float a = bf2f(atile[(16 * j + 4 * (lane >> 4) + e) * 72 + 16 * wave + (lane & 15)]);
        o[e] = (short)f2bf(rbf(bf2f((u16)xv[j][e]) + a) + val);
      }
      *(bf16x4*)(out + (size_t)es * 512 + d0 + 16 * j + 4 * (lane >> 4)) = o;
    }
    STAMPW(tid < 64, 14);
    return;
  }
  const int tid = threadIdx.x - 256, cw = tid >> 6;
  if (mode == 2) {
    __builtin_amdgcn_s_barrier();
    asm volatile("" ::: "memory");
  } else {
  const int ch = cw >> 1, hf = cw & 1, tp = tid & 127;
  const int dch = blockIdx.x * 2 + ch;
  char* cvb = smem + CV_OFF + ch * CV_CH;
  unsigned* pc = cnts + 8 + ch * 4;
  const int half = lane >> 5, m = lane & 31;
  f32x4 r_ex[2];
  f32x2 r_ec[2];
#pragma unroll
  for (int ps = 0; ps < 2; ++ps) {
    const int n = dch * 128 + hf * 64 + ps * 32 + m;
    r_ex[ps] = EX[2 * n + half]; r_ec[ps] = EC[n];
  }
  const float skv = bf2f(dskip[dch]);
  const u16* up = xnT + (size_t)dch * 2048;
  u32x4 uw[4];
  uw[0] = *(const u32x4*)(up + tp * 8);
  uw[1] = *(const u32x4*)(up + tp * 8 + 8);
  uw[2] = *(const u32x4*)(up + 1024 + tp * 8);
  uw[3] = u32x4{0u, 0u, 0u, 0u};
  if (tp < 127) uw[3] = *(const u32x4*)(up + 1024 + tp * 8 + 8);
  const int pk = tp >> 5, pxx = (tp & 31) - 32;
  u16 padv[2];
#pragma unroll
  for (int i = 0; i < 2; ++i) padv[i] = (pxx + pk + 4 * i + 1 >= 0) ? up[pxx + pk + 4 * i + 1] : (u16)0;
  __builtin_amdgcn_sched_barrier(0);
  __builtin_amdgcn_s_barrier();
  asm volatile("" ::: "memory");
  __builtin_amdgcn_s_setprio(KG_PRIO);
  int kix[16], kpi[4], kp2[4];
  {
    const int j = lane & 15, g = lane >> 4;
#pragma unroll
    for (int i = 0; i < 16; ++i) kix[i] = idx_of_l(2047 - 8 * (tp + 128 * (i >> 3)) - (i & 7));
#pragma unroll
    for (int i = 0; i < 4; ++i) {
      const int i0 = 4 * (g & 1) + i + 128 * (g >> 1) + 8 * j;
      const int i1 = (g < 2) ? 256 + i + 64 * g + 4 * j : 384 + (i & 1) + 32 * (i >> 1) + 64 * (g & 1) + 2 * j;
      kpi[i] = hf ? i1 : i0;
      const int i2 = (g < 2) ? 512 + 16 * (4 * g + i) + j : ((g == 2 && i == 0 && j == 0) ? 640 : 644);
      kp2[i] = ((i >> 1) == hf) ? i2 : 645;
    }
#pragma unroll
    for (int i = 0; i < 16; ++i) asm volatile("" : "+v"(kix[i]));
#pragma unroll
    for (int i = 0; i < 4; ++i) asm volatile("" : "+v"(kpi[i]), "+v"(kp2[i]));
    __builtin_amdgcn_sched_barrier(0);
  }
  {
    u16* cp = (u16*)cvb;
    char* tab0 = cvb + 16 * CP_STRIDE;
    char* tab1 = smem + XB_OFF + ch * XB_CH;
    char* tab = hf ? tab1 : tab0;
    float* Kp = (float*)(tab1 + 9216);
    char* wp = tab + half * (16 * 288) + m * 8;
    const char* rp = tab + (lane & 15) * 288 + (lane >> 4) * 16;
    unsigned m0b = 0x0000bf80u, m1b = 0xbf800000u;
    asm volatile("" : "+s"(m0b), "+s"(m1b));
    const bf16x2 m0 = __builtin_bit_cast(bf16x2, m0b), m1 = __builtin_bit_cast(bf16x2, m1b);
    f32x2 tp0[8], tt[8][3];
#pragma unroll
    for (int ps = 0; ps < 2; ++ps) {
      const f32x2 Cc = r_ec[ps];
      const f32x2 X = r_ex[ps].xy, Y = r_ex[ps].zw;
      const f32x2 X2 = cmul2(X, X), X4 = cmul2(X2, X2), Y2 = cmul2(Y, Y), Y4 = cmul2(Y2, Y2);
      const f32x2 s1 = half ? X : X2;
      const f32x2 r1 = {swap32(s1.x), swap32(s1.y)}, r2 = {swap32(X4.x), swap32(X4.y)};
      const f32x2 cx2 = cmul2(Cc, X2), cy = cmul2(Cc, Y);
      tp0[4 * ps] = half ? cx2 : f32x2{1.f, 0.f};
      tp0[4 * ps + 1] = cmul2(half ? Cc : Y4, half ? X4 : Y4);
      tp0[4 * ps + 2] = half ? cy : Cc;
      tp0[4 * ps + 3] = cmul2(Cc, half ? Y2 : r1);
      tt[4 * ps][0] = half ? r1 : Y; tt[4 * ps][1] = half ? r2 : Y2; tt[4 * ps][2] = half ? X : Y4;
      tt[4 * ps + 1][0] = half ? r2 : Y; tt[4 * ps + 1][1] = half ? X : Y2; tt[4 * ps + 1][2] = half ? X2 : Y4;
#pragma unroll
      for (int i = 2; i < 4; ++i) { tt[4 * ps + i][0] = X; tt[4 * ps + i][1] = X2; tt[4 * ps + i][2] = X4; }
    }
    f32x2 P[2][8];
    auto node = [&](int k, int r) {
      if (k < 8 && r < 8) {
        const int lv = r >= 4 ? 2 : (r >= 2 ? 1 : 0);
        P[k & 1][r] = cmul2(P[k & 1][r - (1 << lv)], tt[k][lv]);
      }
    };
    P[0][0] = tp0[0];
#pragma unroll
    for (int r = 1; r < 8; ++r) node(0, r);
    f32x4 acc[3][2] = {};
    u32x4 bq[4], bs[4], aq[4], a2q[4];
    auto mf = [&](int rnd, int ks) {
      if (rnd < 0) return;
      const bf16x8 bh = __builtin_bit_cast(bf16x8, bq[ks]);
      if (!(rnd & 1)) bs[ks] = u32x4{bq[ks][1], bq[ks][0], bq[ks][3], bq[ks][2]};
      const bf16x8 bl = __builtin_bit_cast(bf16x8, bs[ks]);
      const int T = (rnd & 1) ? 2 : 1;
      const bf16x8 a1 = __builtin_bit_cast(bf16x8, a2q[ks]);
      acc[T][0] = __builtin_amdgcn_mfma_f32_16x16x32_bf16(a1, bh, acc[T][0], 0, 0, 0);
      acc[T][1] = __builtin_amdgcn_mfma_f32_16x16x32_bf16(a1, bl, acc[T][1], 0, 0, 0);
      if (rnd & 1) {
        const bf16x8 a0 = __builtin_bit_cast(bf16x8, aq[ks]);
        acc[0][0] = __builtin_amdgcn_mfma_f32_16x16x32_bf16(a0, bh, acc[0][0], 0, 0, 0);
        acc[0][1] = __builtin_amdgcn_mfma_f32_16x16x32_bf16(a0, bl, acc[0][1], 0, 0, 0);
      }
    };
    __builtin_amdgcn_sched_barrier(0);
#pragma unroll
    for (int k = 0; k < 8; ++k) {
      if (k + 1 < 8) P[(k + 1) & 1][0] = tp0[k + 1];
#pragma unroll
      for (int g = 0; g < 4; ++g) {
        bf16x2 h[2];
        f32x2 lv[2];
#pragma unroll
        for (int i = 0; i < 2; ++i) h[i] = __builtin_convertvector(P[k & 1][2 * g + i], bf16x2);
#pragma unroll
        for (int i = 0; i < 2; ++i)
          lv[i] = f32x2{__builtin_amdgcn_fdot2_f32_bf16(h[i], m0, P[k & 1][2 * g + i].x, false),
                        __builtin_amdgcn_fdot2_f32_bf16(h[i], m1, P[k & 1][2 * g + i].y, false)};
        __builtin_amdgcn_sched_barrier(0);
        node(k + 1, 2 * g + 1); node(k + 1, 2 * g + 2);
        __builtin_amdgcn_sched_barrier(0);
        if (k & 1) mf((k >> 1) - 1, g);
        __builtin_amdgcn_sched_barrier(0);
#pragma unroll
        for (int i = 0; i < 2; ++i) {
          const bf16x2 l = __builtin_convertvector(lv[i], bf16x2);
          *(u32x2*)(wp + ((k & 1) * 8 + 2 * g + i) * 288) = u32x2{__builtin_bit_cast(unsigned, h[i]), __builtin_bit_cast(unsigned, l)};
        }
        if (g & 1) {
          const int q = g >> 1, sh = k + 1;
          const unsigned dw[8] = {uw[2 * q][0], uw[2 * q][1], uw[2 * q][2], uw[2 * q][3], uw[2 * q + 1][0], uw[2 * q + 1][1], uw[2 * q + 1][2], uw[2 * q + 1][3]};
          u32x4 o;
#pragma unroll
          for (int j = 0; j < 4; ++j)
            o[j] = (sh & 1) ? __builtin_amdgcn_alignbyte(dw[j + (sh + 1) / 2], dw[j + (sh - 1) / 2], 2) : dw[j + sh / 2];
          *(u32x4*)(cp + k * CP_STRIDE + 32 + 1024 * q + tp * 8) = o;
        }
        __builtin_amdgcn_sched_barrier(0);
      }
      if (k & 1) {
        asm volatile("" ::: "memory");
#pragma unroll
        for (int ks = 0; ks < 4; ++ks) {
          if (k & 2) aq[ks] = *(const u32x4*)(rp + ks * 64); else bq[ks] = *(const u32x4*)(rp + ks * 64);
          a2q[ks] = *(const u32x4*)(rp + 16 * 288 + ks * 64);
        }
        asm volatile("" ::: "memory");
        __builtin_amdgcn_sched_barrier(0);
      }
    }
#pragma unroll
    for (int i = 0; i < 2; ++i) cp[(pk + 4 * i) * CP_STRIDE + 32 + pxx] = padv[i];
#pragma unroll
    for (int ks = 0; ks < 4; ++ks) mf(3, ks);
    asm volatile("" ::: "memory");
#pragma unroll
    for (int T = 0; T < 3; ++T) *(f32x4*)(tab + T * 1024 + lane * 16) = acc[T][0] + acc[T][1];
    team_barrier(pc + 1, 2u, lane);
    STAMP(tid < 64, 5);
    {
      const f32x4 v = *(const f32x4*)(tab0 + hf * 1024 + lane * 16) + *(const f32x4*)(tab1 + hf * 1024 + lane * 16);
      const f32x4 v2 = *(const f32x4*)(tab0 + 2048 + lane * 16) + *(const f32x4*)(tab1 + 2048 + lane * 16);
#pragma unroll
      for (int i = 0; i < 4; ++i) {
        Kp[kpi[i]] = 2.0f * v[i];
        Kp[kp2[i]] = 2.0f * v2[i];
      }
    }
    team_barrier(pc + 2, 2u, lane);
    u16* kr = (u16*)cvb + 8 * CP_STRIDE;
#pragma unroll
    for (int q = 0; q < 2; ++q) {
      bf16x8 kh, kl;
#pragma unroll
      for (int e = 0; e < 8; ++e) {
        const float kv = Kp[kix[8 * q + e]];
        const u16 hv = f2bf(kv);
        kh[e] = (short)hv;
        kl[e] = (short)f2bf(kv - bf2f(hv));
      }
      *(bf16x8*)(kr + (tp + 128 * q) * 8) = kh;
      *(bf16x8*)(kr + KR_LEN + (tp + 128 * q) * 8) = kl;
    }
    if (tp < 60) {
      const int tb = tp >= 30 ? 1 : 0;
      *(u32x4*)(kr + tb * KR_LEN + 2048 + (tp - 30 * tb) * 8) = u32x4{0u, 0u, 0u, 0u};
    }
  }
  team_barrier(pc + 3, 2u, lane);
  __builtin_amdgcn_s_setprio(CV_PRIO);
  STAMP(tid < 64, 6);
  const u16* cpc = (const u16*)(smem + CV_OFF + ch * CV_CH);
  f32x4 acc[8] = {};
  {
    const int r = lane & 15, kg = lane >> 4;
    const u16* ub = cpc + (r & 7) * CP_STRIDE + 8 * (r >> 3) + 8 * kg + 16 + 128 * hf;
    const u16* kb = cpc + 8 * CP_STRIDE + 2032 - 16 * r + 8 * kg + 128 * hf;
    bf16x8 U[2][8], Kf[2][4];
    unsigned pz = __hip_atomic_load(cnts + 7, __ATOMIC_RELAXED, __HIP_MEMORY_SCOPE_WORKGROUP);
#pragma unroll
    for (int j = 0; j < 8; ++j) U[0][j] = *(const bf16x8*)(ub + 256 * j);
    Kf[0][0] = *(const bf16x8*)(kb); Kf[0][1] = *(const bf16x8*)(kb + KR_LEN);
    Kf[0][2] = *(const bf16x8*)(kb - 256 * 7); Kf[0][3] = *(const bf16x8*)(kb - 256 * 7 + KR_LEN);
#pragma unroll
    for (int su = 0; su < 16; ++su) {
      const int ti = su >> 2, pp = su & 3;
      if (!(su & 1)) {
        unsigned z = __builtin_amdgcn_readfirstlane(pz);
        while (z != 0u && z < 64u) {
          __builtin_amdgcn_s_sleep(1);
          z = __builtin_amdgcn_readfirstlane(__hip_atomic_load(cnts + 7, __ATOMIC_RELAXED, __HIP_MEMORY_SCOPE_WORKGROUP));
        }
      }
      if (su + 1 < 16) {
        if (su & 1) pz = __hip_atomic_load(cnts + 7, __ATOMIC_RELAXED, __HIP_MEMORY_SCOPE_WORKGROUP);
        const int ti2 = (su + 1) >> 2, p2 = (su + 1) & 3;
        const u16* k1 = kb - 256 * p2 + 32 * ti2;
        const u16* k2 = kb - 256 * (7 - p2) + 32 * ti2;
        Kf[(su + 1) & 1][0] = *(const bf16x8*)(k1); Kf[(su + 1) & 1][1] = *(const bf16x8*)(k1 + KR_LEN);
        Kf[(su + 1) & 1][2] = *(const bf16x8*)(k2); Kf[(su + 1) & 1][3] = *(const bf16x8*)(k2 + KR_LEN);
      }
      if (ti < 3) {
        U[(ti + 1) & 1][2 * pp] = *(const bf16x8*)(ub + 32 * (ti + 1) + 256 * (2 * pp));
        U[(ti + 1) & 1][2 * pp + 1] = *(const bf16x8*)(ub + 32 * (ti + 1) + 256 * (2 * pp + 1));
      }
#pragma unroll
      for (int j = 0; j < 8 - pp; ++j) {
        acc[j + pp] = __builtin_amdgcn_mfma_f32_16x16x32_bf16(U[ti & 1][j], Kf[su & 1][0], acc[j + pp], 0, 0, 0);
        acc[j + pp] = __builtin_amdgcn_mfma_f32_16x16x32_bf16(U[ti & 1][j], Kf[su & 1][1], acc[j + pp], 0, 0, 0);
      }
#pragma unroll
      for (int j = 0; j <= pp; ++j) {
        acc[j + 7 - pp] = __builtin_amdgcn_mfma_f32_16x16x32_bf16(U[ti & 1][j], Kf[su & 1][2], acc[j + 7 - pp], 0, 0, 0);
        acc[j + 7 - pp] = __builtin_amdgcn_mfma_f32_16x16x32_bf16(U[ti & 1][j], Kf[su & 1][3], acc[j + 7 - pp], 0, 0, 0);
      }
#pragma unroll
      for (int i = 0; i < 7; ++i) {
        __builtin_amdgcn_sched_group_barrier(0x008, 1, 0);
        __builtin_amdgcn_sched_group_barrier(0x100, 1, 0);
      }
      __builtin_amdgcn_sched_group_barrier(0x008, 11, 0);
      __builtin_amdgcn_sched_barrier(0);
    }
  }
  {
    STAMP(tid < 64, 7);
    __builtin_amdgcn_s_setprio(2);
    f32x4* xo = (f32x4*)(smem + XB_OFF + ch * XB_CH + hf * 4096);
    const f32x4* xi = (const f32x4*)(smem + XB_OFF + ch * XB_CH + (hf ^ 1) * 4096);
    f32x4 keep[4];
#pragma unroll
    for (int q4 = 0; q4 < 4; ++q4) {
      f32x4 lo = acc[q4], hi = acc[4 + q4];
      asm volatile("" : "+v"(lo), "+v"(hi));
      xo[q4 * 64 + lane] = hf ? lo : hi;
      keep[q4] = hf ? hi : lo;
    }
    team_barrier(cnts + 8 + ch * 4, 2u, lane);
    const int d = blockIdx.x * 2 + ch;
#pragma unroll
    for (int q4 = 0; q4 < 4; ++q4) {
      const f32x4 v4 = keep[q4] + xi[q4 * 64 + lane];
      const int l = 256 * (4 * hf + q4) + 16 * (lane & 15) + 4 * (lane >> 4);
      const bf16x4 u4 = *(const bf16x4*)(cpc + 7 * CP_STRIDE + 24 + l);
      bf16x4 o;
#pragma unroll
      for (int e = 0; e < 4; ++e) o[e] = (short)f2bf(rbf(v4[e]) + rbf(bf2f((u16)u4[e]) * skv));
      st8_agent(aT + (size_t)d * 2048 + l, o);
    }
    WAIT_VM(0);
    if (lane == 0) {
      if (lds_inc(cnts + 6) == 3u) __hip_atomic_store(flags + (256 + blockIdx.x) * FLS, 1u, __ATOMIC_RELAXED, __HIP_MEMORY_SCOPE_AGENT);
    }
    STAMP(tid < 64, 9);
  }
  if (mode == 1) return;
  }
  if (cw) return;
  while (true) {
    unsigned v = __hip_atomic_load(cnts + 3, __ATOMIC_RELAXED, __HIP_MEMORY_SCOPE_WORKGROUP);
    if (__builtin_amdgcn_readfirstlane(v) >= 1u) break;
    __builtin_amdgcn_s_sleep(8);
  }
  if (mode == 0) team_wait(cnts + 15, 2u);
  poll_flags<8>(flags + (256 + bn2 * 32 + lane) * FLS, lane < 32);
  asm volatile("" ::: "memory");
  {
    const int row = lane >> 4;
    unsigned long long av[16];
#pragma unroll
    for (int i = 0; i < 16; ++i)
      av[i] = __hip_atomic_load((const unsigned long long*)(aT + (size_t)(d0 + row + 4 * i) * 2048 + s0 + (lane & 15) * 4), __ATOMIC_RELAXED, __HIP_MEMORY_SCOPE_AGENT);
#pragma unroll
    for (int i = 0; i < 16; ++i) *(unsigned long long*)(atile + (row + 4 * i) * 72 + (lane & 15) * 4) = av[i];
  }
  team_arrive(cnts + 5, lane);
  STAMP(tid < 64, 15);
}

extern "C" void kernel_launch(void* const* d_in, const int* in_sizes, int n_in,
                              void* d_out, int out_size, void* d_ws, size_t ws_size,
                              hipStream_t stream) {
  const u16* x = (const u16*)d_in[0];
  const u16* lar = (const u16*)d_in[1];
  const u16* aim = (const u16*)d_in[2];
  const u16* cr = (const u16*)d_in[3];
  const u16* ci = (const u16*)d_in[4];
  const u16* logdt = (const u16*)d_in[5];
  const u16* dskip = (const u16*)d_in[6];
  const u16* w1 = (const u16*)d_in[8];
  const u16* b1 = (const u16*)d_in[9];
  const u16* w2 = (const u16*)d_in[10];
  const u16* b2 = (const u16*)d_in[11];
  u16* out = (u16*)d_out;
  char* ws = (char*)d_ws;
  const size_t SZ = (size_t)2048 * 512 * 2;
  u16* hT = (u16*)(ws);
  u16* xnT = (u16*)(ws + 2 * SZ);
  u16* aT = (u16*)(ws + 3 * SZ);
  u16* G = (u16*)(ws + 6 * SZ);
  unsigned* flags = (unsigned*)(ws + 8 * SZ);
  f32x4* EX = (f32x4*)(ws + 4 * SZ);
  f32x2* EC = (f32x2*)(ws + 5 * SZ);
  int dev = 0, ncu = 0;
  const bool fused = hipGetDevice(&dev) == hipSuccess &&
                     hipDeviceGetAttribute(&ncu, hipDeviceAttributeMultiprocessorCount, dev) == hipSuccess && ncu >= 256;
  prep_kernel<<<512, 256, 0, stream>>>(x, hT, xnT, w1, w2, lar, aim, cr, ci, logdt, EX, EC, flags);
  if (fused) {
    mid_kernel<<<256, 512, 0, stream>>>(hT, w1, b1, G, xnT, EX, EC, dskip, aT, w2, b2, x, out, flags, 0L);
  } else {
    mid_kernel<<<256, 512, 0, stream>>>(hT, w1, b1, G, xnT, EX, EC, dskip, aT, w2, b2, x, out, flags, 1L);
    mid_kernel<<<256, 512, 0, stream>>>(hT, w1, b1, G, xnT, EX, EC, dskip, aT, w2, b2, x, out, flags, 2L);
  }
}
```
